```python
import torch

def _softmax(x, axis=-1):
    return torch.softmax(x, dim=axis)
def _gelu(x):
    return torch.nn.functional.gelu(x, approximate="tanh")
def _silu(x):
    return torch.nn.functional.silu(x)
def _sigmoid(x):
    return torch.sigmoid(x)
def _layernorm(x, axis=-1, eps=1e-5):
    m = x.mean(dim=axis, keepdim=True)
    v = x.var(dim=axis, keepdim=True, unbiased=False)
    return (x - m) / torch.sqrt(v + eps)
def _scatter_add(tgt, idx, src, axis=0):
    return tgt.clone().index_add_(axis, idx, src)
def _segment_sum(src, seg, n):
    out = torch.zeros((n,) + src.shape[1:], dtype=src.dtype)
    return out.index_add_(0, seg, src)
def _pad(x, pw):
    flat = tuple(v for pair in reversed(pw) for v in pair)
    return torch.nn.functional.pad(x, flat)

kernel_name = 'gated_mix__spectral_fnet__swiglu_ffn__prompt'

def setup_inputs():

    _w = (lambda a: (lambda t: t.to(torch.bfloat16) if t.is_floating_point() else t)(torch.as_tensor(a)))
    def _A_setup():
        B, S, D = 1, 2048, 512
        ns = torch.arange(S)[:, None] * torch.arange(S)[None, :]
        nd = torch.arange(D)[:, None] * torch.arange(D)[None, :]
        Ws_re = torch.cos(-2.0 * torch.pi * ns / S).to(float)
        Ws_im = torch.sin(-2.0 * torch.pi * ns / S).to(float)
        Wd_re = torch.cos(-2.0 * torch.pi * nd / D).to(float)
        Wd_im = torch.sin(-2.0 * torch.pi * nd / D).to(float)
        return {
            "x": _w((lambda *s: torch.randn(*s, dtype=torch.bfloat16) * 0.05)(B, S, D)),
            "dft_seq_re": _w(Ws_re),
            "dft_seq_im": _w(Ws_im),
            "dft_hid_re": _w(Wd_re),
            "dft_hid_im": _w(Wd_im),
        }
    _A_in = _A_setup()

    def _B_setup():
        B, S, D, F = 1, 2048, 512, 1792
        return {
            "x": _w((lambda *s: torch.randn(*s, dtype=torch.bfloat16) * 0.05)(B, S, D)),
            "w_gate": _w((lambda *s: torch.randn(*s, dtype=torch.bfloat16) * 0.8)(D, F)),
            "w_up": _w((lambda *s: torch.randn(*s, dtype=torch.bfloat16) * 0.8)(D, F)),
            "w_down": _w((lambda *s: torch.randn(*s, dtype=torch.bfloat16) * 0.8)(F, D)),
        }
    _B_in = _B_setup()

    return {
        "A_x": _A_in["x"],
        "A_dft_seq_re": _A_in["dft_seq_re"],
        "A_dft_seq_im": _A_in["dft_seq_im"],
        "A_dft_hid_re": _A_in["dft_hid_re"],
        "A_dft_hid_im": _A_in["dft_hid_im"],
        "B_x": _B_in["x"],
        "B_w_gate": _B_in["w_gate"],
        "B_w_up": _B_in["w_up"],
        "B_w_down": _B_in["w_down"],
        "gate_w": _w((lambda *s: torch.randn(*s, dtype=torch.bfloat16) * 0.05)(512) * 16.0),
    }

def reference(A_x, A_dft_seq_re, A_dft_seq_im, A_dft_hid_re, A_dft_hid_im, B_x, B_w_gate, B_w_up, B_w_down, gate_w):
    def _A_ref(x, dft_seq_re, dft_seq_im, dft_hid_re, dft_hid_im):
        a = torch.einsum("bsd,ed->bse", x, dft_hid_re)
        b = torch.einsum("bsd,ed->bse", x, dft_hid_im)
        y = torch.einsum("ts,bse->bte", dft_seq_re, a) - torch.einsum("ts,bse->bte", dft_seq_im, b)
        return y

    def _B_ref(x, w_gate, w_up, w_down):
        g = _silu(x @ w_gate)
        u = x @ w_up
        return (g * u) @ w_down

    x = A_x
    g = _sigmoid((x * gate_w).sum(-1, keepdims=True))
    a = _A_ref(x, A_dft_seq_re, A_dft_seq_im, A_dft_hid_re, A_dft_hid_im)
    b = _B_ref(x, B_w_gate, B_w_up, B_w_down)
    return g * a + (1 - g) * b
```

```cpp
#include <hip/hip_runtime.h>
#include <hip/hip_bf16.h>
typedef unsigned short u16;
__device__ __forceinline__ float bf2f(u16 v){ return __uint_as_float(((unsigned)v)<<16); }
__device__ __forceinline__ u16 f2bf(float f){ unsigned u=__float_as_uint(f); u += 0x7fffu + ((u>>16)&1u); return (u16)(u>>16); }
constexpr int S=2048, D=512, F=1792;
__global__ void k_pq(const u16* x, const u16* cr, const u16* ci, u16* pq){
  int d = blockIdx.x*256+threadIdx.x;
  int t = blockIdx.y; int which = blockIdx.z;
  const u16* c = which? ci: cr;
  float acc=0;
  for(int s=0;s<S;++s) acc += bf2f(c[t*S+s])*bf2f(x[s*D+d]);
  pq[t*1024 + which*512 + d] = f2bf(acc);
}
__global__ void k_h(const u16* x, const u16* wg, const u16* wu, u16* h){
  int f = blockIdx.x*256+threadIdx.x; int t = blockIdx.y;
  float g=0,u=0;
  for(int d=0;d<D;++d){ float xv=bf2f(x[t*D+d]); g+=xv*bf2f(wg[d*F+f]); u+=xv*bf2f(wu[d*F+f]); }
  float sg = g/(1.f+__expf(-g));
  h[t*F+f]=f2bf(sg*u);
}
__global__ void k_out(const u16* x, const u16* gw, const u16* pq, const u16* h, const u16* dr, const u16* di, const u16* wd, u16* out){
  int e = blockIdx.x*256+threadIdx.x; int t = blockIdx.y;
  float z=0; for(int d=0;d<D;++d) z += bf2f(x[t*D+d])*bf2f(gw[d]);
  float g = 1.f/(1.f+__expf(-z));
  float y=0; for(int d=0;d<D;++d) y += bf2f(pq[t*1024+d])*bf2f(dr[e*D+d]) - bf2f(pq[t*1024+512+d])*bf2f(di[e*D+d]);
  float b=0; for(int f=0;f<F;++f) b += bf2f(h[t*F+f])*bf2f(wd[f*D+e]);
  out[t*D+e]=f2bf(g*y+(1.f-g)*b);
}
extern "C" void kernel_launch(void* const* d_in, const int* in_sizes, int n_in,
                              void* d_out, int out_size, void* d_ws, size_t ws_size,
                              hipStream_t stream) {
  const u16* x=(const u16*)d_in[0]; const u16* cr=(const u16*)d_in[1]; const u16* ci=(const u16*)d_in[2];
  const u16* dr=(const u16*)d_in[3]; const u16* di=(const u16*)d_in[4];
  const u16* wg=(const u16*)d_in[6]; const u16* wu=(const u16*)d_in[7]; const u16* wd=(const u16*)d_in[8]; const u16* gw=(const u16*)d_in[9];
  u16* pq=(u16*)d_ws; u16* h=pq+2048*1024;
  k_pq<<<dim3(2,S,2),256,0,stream>>>(x,cr,ci,pq);
  k_h<<<dim3(7,S),256,0,stream>>>(x,wg,wu,h);
  k_out<<<dim3(2,S),256,0,stream>>>(x,gw,pq,h,dr,di,wd,(u16*)d_out);
}
```

```cpp
#include <hip/hip_runtime.h>
#include <hip/hip_bf16.h>

__global__ __launch_bounds__(512, 2) __attribute__((aligned(4096))) void fused_kernel(const void* x, const void* cr, const void* ci, const void* dr,
                                                       const void* di, const void* wg, const void* wu, const void* wd,
                                                       const void* gw, void* out, void* ws) {
  __shared__ __attribute__((aligned(16))) char lds[163840];
  const unsigned lds0 = (unsigned)(size_t)(__attribute__((address_space(3))) char*)lds;
  const unsigned wave = __builtin_amdgcn_readfirstlane(threadIdx.x >> 6);
  asm volatile(
    ".LSTART_%=:\n"
    "s_load_dwordx16 s[8:23], %0, 0x0\n"
    "s_load_dwordx4 s[24:27], %0, 0x40\n"
    "s_load_dwordx2 s[28:29], %0, 0x50\n"
    "s_mov_b32 s30, %1\n"
    "s_xor_b32 s31, %2, 4\n"
    "s_and_b32 s34, s30, 3\n"
    "s_bfe_u32 s35, s30, 0x20003\n"
    "s_lshr_b32 s70, s30, 5\n"
    "s_and_b32 s71, s30, 4\n"
    "s_lshl1_add_u32 s70, s71, s70\n"
    "s_and_b32 s74, s31, 3\n"
    "s_lshr_b32 s75, s74, 1\n"
    "s_and_b32 s76, s74, 1\n"
    "v_mbcnt_lo_u32_b32 v240, -1, 0\n"
    "v_mbcnt_hi_u32_b32 v240, -1, v240\n"
    "s_cmp_eq_u32 s31, 0\n"
    "s_cbranch_scc0 .LCNT_%=\n"
    "s_getpc_b64 s[72:73]\n"
    ".LCPC_%=:\n"
    "s_sub_u32 s72, s72, .LCPC_%=-.LSTART_%=\n"
    "s_subb_u32 s73, s73, 0\n"
    "s_lshr_b32 s71, s30, 3\n"
    "s_mul_i32 s71, s71, 10\n"
    "v_min_u32 v243, 9, v240\n"
    "v_add_u32 v243, s71, v243\n"
    "v_lshlrev_b32 v243, 7, v243\n"
    "v_min_u32 v243, .LEND_%=-.LSTART_%=-4, v243\n"
    "global_load_dword v251, v243, s[72:73]\n"
    ".LCNT_%=:\n"
    "s_lshl2_add_u32 s71, s31, s35\n"
    "s_and_b32 s71, s71, 0x13\n"
    "s_cmp_eq_u32 s71, 3\n"
    "s_cbranch_scc1 .LAZ_%=\n"
    "s_cmp_lt_u32 s71, 3\n"
    "s_cbranch_scc1 .LAX_%=\n"
    "s_setprio 3\n"
    "s_and_b32 s71, s74, 3\n"
    "s_lshl_b32 s71, s71, 3\n"
    "s_and_b32 s72, s34, 2\n"
    "s_lshl_b32 s72, s72, 18\n"
    "s_add_u32 s72, s72, 0x40183028\n"
    "s_lshr_b32 s71, s72, s71\n"
    "s_and_b32 s71, s71, 0xff\n"
    "s_load_dwordx2 s[68:69], %0, s71\n"
    "s_mov_b32 s38, 2097152\n"
    "s_mov_b32 s39, 0x20000\n"
    "s_mov_b32 s43, 0x20000\n"
    "s_mov_b32 s47, 0x20000\n"
    "s_mov_b32 s51, 0x20000\n"
    "s_mov_b32 s83, 0x20000\n"
    "s_mov_b32 s87, 0x20000\n"
    "s_mov_b32 s42, 524288\n"
    "s_mov_b32 s46, 1835008\n"
    "s_mov_b32 s50, 1835008\n"
    "v_lshrrev_b32 v192, 3, v240\n"
    "v_lshrrev_b32 v194, 4, v240\n"
    "v_xor_b32 v193, v194, v240\n"
    "v_and_b32 v193, 7, v193\n"
    "v_lshlrev_b32 v193, 4, v193\n"
    "v_lshrrev_b32 v195, 5, v240\n"
    "v_and_b32 v196, 3, v192\n"
    "v_lshl_add_u32 v195, v195, 3, v196\n"
    "v_lshl_add_u32 v195, v195, 10, v193\n"
    "s_lshl2_add_u32 s71, s70, s74\n"
    "s_lshl_b32 s71, s71, 15\n"
    "v_add_u32 v0, s71, v195\n"
    "v_xor_b32 v1, 16448, v0\n"
    "v_xor_b32 v2, 4096, v0\n"
    "v_xor_b32 v3, 20544, v0\n"
    "s_xor_b32 s77, s70, s35\n"
    "s_and_b32 s77, s77, 3\n"
    "s_lshl_b32 s95, s74, 12\n"
    "v_lshl_add_u32 v195, v192, 10, v193\n"
    "s_and_b32 s71, s34, 1\n"
    "s_lshl3_add_u32 s71, s71, s35\n"
    "s_lshl_b32 s88, s71, 15\n"
    "s_mul_i32 s89, s34, 896\n"
    "s_lshl_b32 s71, s35, 8\n"
    "s_add_u32 s89, s89, s71\n"
    "s_mul_i32 s71, s74, 57344\n"
    "s_add_u32 s89, s89, s71\n"
    "s_lshl_b32 s71, s76, 6\n"
    "v_xor_b32 v4, s71, v195\n"
    "s_lshl_b32 s71, s74, 13\n"
    "s_add_u32 s90, s71, s88\n"
    "s_lshl_b32 s96, s74, 10\n"
    "s_cmp_eq_u32 s35, 3\n"
    "s_cbranch_scc1 .LBZ_%=\n"
    "v_add_u32 v4, s90, v4\n"
    "v_lshlrev_b32 v195, 1, v194\n"
    "v_xor_b32 v195, v195, v240\n"
    "v_and_b32 v195, 15, v195\n"
    "v_lshlrev_b32 v195, 4, v195\n"
    "v_xor_b32 v196, 128, v195\n"
    "v_mul_u32_u24 v194, 3584, v194\n"
    "v_add3_u32 v6, v194, v195, s89\n"
    "v_add_u32 v7, 14336, v6\n"
    "s_add_u32 s89, s89, 28672\n"
    "v_add3_u32 v8, v194, v196, s89\n"
    "v_add_u32 v9, 14336, v8\n"
    "s_add_u32 s97, s95, 16384\n"
    "s_add_u32 s98, s95, 32768\n"
    "s_add_u32 s99, s95, 18432\n"
    "s_mov_b32 s92, 229376\n"
    "s_mov_b32 s93, 0\n"
    "s_branch .LBJ_%=\n"
    ".LRET_%=:\n"
    "s_setpc_b64 s[18:19]\n"
    ".LBZ_%=:\n"
    "s_add_u32 s90, s90, 131072\n"
    "v_add_u32 v4, s90, v4\n"
    "s_lshl_b32 s71, s74, 15\n"
    "s_add_u32 s71, s71, s88\n"
    "v_add_u32 v8, s71, v195\n"
    "v_xor_b32 v9, 8256, v8\n"
    "v_and_b32 v195, 2, v192\n"
    "v_xor_b32 v195, v195, v240\n"
    "v_and_b32 v195, 7, v195\n"
    "v_lshlrev_b32 v195, 4, v195\n"
    "v_xor_b32 v196, 64, v195\n"
    "v_mul_u32_u24 v194, 3584, v192\n"
    "v_add3_u32 v6, v194, v195, s89\n"
    "s_add_u32 s89, s89, 28672\n"
    "v_add3_u32 v7, v194, v196, s89\n"
    "s_lshl_b32 s71, s74, 11\n"
    "s_add_u32 s97, s71, 16384\n"
    "s_add_u32 s98, s71, 24576\n"
    "s_add_u32 s99, s95, 32768\n"
    "s_movk_i32 s92, 128\n"
    "s_mov_b32 s93, 16384\n"
    ".LBJ_%=:\n"
    "s_and_b32 s94, s77, 1\n"
    "s_lshl_b32 s88, s94, 7\n"
    "s_mul_i32 s89, s94, 229376\n"
    "s_mul_i32 s90, s94, s92\n"
    "s_add_u32 s91, s90, s93\n"
    "s_waitcnt lgkmcnt(0)\n"
    "s_load_dword s5, s[68:69], 0x0\n"
    "s_mov_b32 s36, s8\n"
    "s_and_b32 s37, s9, 0xffff\n"
    "s_add_u32 m0, s95, 0\n"
    "s_nop 0\n"
    "buffer_load_dwordx4 v0, s[36:39], s88 offen lds\n"
    "s_add_u32 m0, s95, 1024\n"
    "s_nop 0\n"
    "buffer_load_dwordx4 v1, s[36:39], s88 offen lds\n"
    "s_add_u32 m0, s95, 2048\n"
    "s_nop 0\n"
    "buffer_load_dwordx4 v2, s[36:39], s88 offen lds\n"
    "s_add_u32 m0, s95, 3072\n"
    "s_nop 0\n"
    "buffer_load_dwordx4 v3, s[36:39], s88 offen lds\n"
    "s_cmp_ge_u32 s34, 2\n"
    "s_cselect_b64 s[40:41], s[16:17], s[14:15]\n"
    "s_and_b32 s41, s41, 0xffff\n"
    "s_add_u32 m0, s96, 49152\n"
    "s_nop 0\n"
    "buffer_load_dwordx4 v4, s[40:43], s88 offen lds\n"
    "s_mov_b32 s44, s18\n"
    "s_and_b32 s45, s19, 0xffff\n"
    "s_mov_b32 s48, s20\n"
    "s_and_b32 s49, s21, 0xffff\n"
    "s_cmp_eq_u32 s35, 3\n"
    "s_cselect_b64 s[80:81], s[40:41], s[44:45]\n"
    "s_cselect_b64 s[84:85], s[40:41], s[48:49]\n"
    "s_cselect_b32 s82, s42, s46\n"
    "s_mov_b32 s86, s82\n"
    "s_add_u32 m0, s99, 0\n"
    "s_nop 0\n"
    "buffer_load_dwordx4 v8, s[80:83], s90 offen lds\n"
    "s_add_u32 m0, s99, 1024\n"
    "s_nop 0\n"
    "buffer_load_dwordx4 v9, s[80:83], s90 offen lds\n"
    "s_add_u32 m0, s97, 0\n"
    "s_nop 0\n"
    "buffer_load_dwordx4 v6, s[44:47], s89 offen lds\n"
    "s_add_u32 m0, s97, 1024\n"
    "s_nop 0\n"
    "buffer_load_dwordx4 v7, s[44:47], s89 offen lds\n"
    "s_add_u32 s72, s28, 22020096\n"
    "s_addc_u32 s73, s29, 0\n"
    "s_load_dwordx2 s[68:69], s[72:73], 0x0\n"
    "s_cmp_ge_u32 s34, 2\n"
    "s_cselect_b64 s[52:53], s[12:13], s[10:11]\n"
    "s_lshl2_add_u32 s71, s70, s74\n"
    "s_lshl_b32 s72, s71, 17\n"
    "s_load_dword s5, s[52:53], s72\n"
    "s_lshl2_add_u32 s72, s34, s74\n"
    "s_mul_i32 s72, s72, 114688\n"
    "s_load_dword s5, s[22:23], s72\n"
    "v_lshlrev_b32 v243, 4, v240\n"
    "s_mov_b32 s54, 8388608\n"
    "s_mov_b32 s55, 0x20000\n"
    "s_cmp_ge_u32 s34, 2\n"
    "s_cselect_b64 s[52:53], s[12:13], s[10:11]\n"
    "s_and_b32 s53, s53, 0xffff\n"
    "s_mov_b32 s58, 1835008\n"
    "s_mov_b32 s59, 0x20000\n"
    "s_mov_b32 s56, s22\n"
    "s_and_b32 s57, s23, 0xffff\n"
    "s_mov_b32 s62, 25165824\n"
    "s_mov_b32 s63, 0x20000\n"
    "s_mov_b32 s60, s28\n"
    "s_and_b32 s61, s29, 0xffff\n"
    "s_mov_b32 s66, 2097152\n"
    "s_mov_b32 s67, 0x20000\n"
    "s_mov_b32 s64, s26\n"
    "s_and_b32 s65, s27, 0xffff\n"
    "s_lshl2_add_u32 s71, s70, s34\n"
    "s_and_b32 s72, s35, 1\n"
    "s_lshl1_add_u32 s71, s71, s72\n"
    "s_lshl1_add_u32 s71, s71, s75\n"
    "s_lshr_b32 s72, s35, 1\n"
    "s_lshl1_add_u32 s71, s71, s72\n"
    "s_lshl1_add_u32 s71, s71, s76\n"
    "s_lshl_b32 s78, s71, 11\n"
    "s_lshl2_add_u32 s71, s70, s35\n"
    "s_lshl2_add_u32 s71, s71, s74\n"
    "s_and_b32 s72, s34, 1\n"
    "s_lshl4_add_u32 s71, s71, s72\n"
    "s_lshl_b32 s71, s71, 11\n"
    "v_and_b32 v195, 7, v240\n"
    "v_lshl_add_u32 v195, v192, 5, v195\n"
    "v_lshl_add_u32 v36, v195, 7, s71\n"
    "s_lshl2_add_u32 s71, s35, s74\n"
    "s_mul_i32 s71, s71, 22\n"
    "v_min_u32 v194, 21, v240\n"
    "v_add_u32 v194, s71, v194\n"
    "v_min_u32 v194, 255, v194\n"
    "v_lshrrev_b32 v195, 3, v194\n"
    "v_and_b32 v194, 7, v194\n"
    "v_lshl_add_u32 v194, v195, 5, v194\n"
    "s_lshl2_add_u32 s71, s70, 3\n"
    "s_lshl_b32 s71, s71, 6\n"
    "s_add_u32 s71, s71, s72\n"
    "s_lshl_b32 s71, s71, 11\n"
    "v_lshl_add_u32 v38, v194, 7, s71\n"
    "s_lshr_b32 s71, s30, 2\n"
    "s_lshl2_add_u32 s71, s71, s74\n"
    "s_lshl_b32 s72, s34, 8\n"
    "s_add_u32 s71, s71, s72\n"
    "s_mul_i32 s71, s71, 1792\n"
    "v_min_u32 v194, 13, v240\n"
    "v_lshl_add_u32 v37, v194, 7, s71\n"
    "v_lshrrev_b32 v241, 4, v240\n"
    "v_and_b32 v242, 15, v240\n"
    "v_lshrrev_b32 v194, 1, v242\n"
    "v_xor_b32 v194, v194, v241\n"
    "v_lshlrev_b32 v194, 4, v194\n"
    "v_lshl_add_u32 v194, v242, 7, v194\n"
    "s_lshl_b32 s71, s75, 13\n"
    "s_add_u32 s71, s71, 65536\n"
    "v_add_u32 v220, s71, v194\n"
    "v_xor_b32 v221, 64, v220\n"
    "v_add_u32 v222, 49152, v220\n"
    "v_add_u32 v223, 49152, v221\n"
    "v_add_u32 v226, s71, v243\n"
    "v_add_u32 v227, 49152, v226\n"
    "s_lshl_b32 s71, s76, 13\n"
    "v_add_u32 v224, s71, v243\n"
    "v_lshrrev_b32 v195, 2, v242\n"
    "v_and_b32 v194, 1, v241\n"
    "v_xor_b32 v194, s76, v194\n"
    "v_lshl_add_u32 v194, v194, 2, v195\n"
    "v_and_b32 v196, 3, v240\n"
    "v_lshl_add_u32 v194, v194, 2, v196\n"
    "v_lshl_add_u32 v195, v241, 3, v195\n"
    "v_lshl_add_u32 v194, v195, 5, v194\n"
    "v_lshlrev_b32 v228, 3, v194\n"
    "v_xor_b32 v229, 32, v228\n"
    "v_xor_b32 v230, 64, v228\n"
    "v_xor_b32 v231, 96, v228\n"
    "s_lshl_b32 s71, s75, 8\n"
    "s_add_u32 s71, s71, 107008\n"
    "v_lshl_add_u32 v235, v242, 2, s71\n"
    "v_lshrrev_b32 v216, 4, v240\n"
    "v_xor_b32 v216, v216, v240\n"
    "v_and_b32 v216, 7, v216\n"
    "v_lshlrev_b32 v216, 4, v216\n"
    "v_lshrrev_b32 v217, 5, v240\n"
    "v_bfe_u32 v218, v240, 3, 2\n"
    "v_lshl_add_u32 v217, v217, 3, v218\n"
    "v_lshl_add_u32 v216, v217, 12, v216\n"
    "v_xor_b32 v217, 0x10040, v216\n"
    "s_lshl1_add_u32 s71, s74, 0\n"
    "s_xor_b32 s71, s71, s34\n"
    "s_lshr_b32 s72, s71, 1\n"
    "s_lshl2_add_u32 s72, s70, s72\n"
    "s_and_b32 s71, s71, 1\n"
    "s_lshl3_add_u32 s71, s72, s71\n"
    "s_and_b32 s72, s34, 1\n"
    "s_lshl3_add_u32 s71, s71, s72\n"
    "s_lshl_b32 s71, s71, 11\n"
    "v_add_u32 v204, s71, v216\n"
    "v_add_u32 v205, s71, v217\n"
    "s_lshl1_add_u32 s71, s74, 1\n"
    "s_xor_b32 s71, s71, s34\n"
    "s_lshr_b32 s72, s71, 1\n"
    "s_lshl2_add_u32 s72, s70, s72\n"
    "s_and_b32 s71, s71, 1\n"
    "s_lshl3_add_u32 s71, s72, s71\n"
    "s_and_b32 s72, s34, 1\n"
    "s_lshl3_add_u32 s71, s71, s72\n"
    "s_lshl_b32 s71, s71, 11\n"
    "v_add_u32 v206, s71, v216\n"
    "v_add_u32 v207, s71, v217\n"
    "s_lshl_b32 s71, s34, 6\n"
    "s_add_u32 s71, s71, s35\n"
    "s_xor_b32 s71, s71, 3\n"
    "s_lshl2_add_u32 s71, s71, s74\n"
    "s_lshl_b32 s71, s71, 12\n"
    "v_lshl_add_u32 v208, v240, 4, s71\n"
    "s_xor_b32 s71, s35, 3\n"
    "s_and_b32 s71, s71, 1\n"
    "s_lshl2_add_u32 s71, s71, s74\n"
    "s_min_u32 s71, s71, 3\n"
    "s_add_u32 m0, s95, 34816\n"
    "s_nop 0\n"
    "buffer_load_dwordx4 v8, s[84:87], s91 offen lds\n"
    "s_lshl2_add_u32 s71, s30, s71\n"
    "s_lshl_b32 s71, s71, 7\n"
    "s_add_u32 s71, s71, 20971520\n"
    "v_lshl_add_u32 v197, v240, 3, s71\n"
    "v_xor_b32 v195, s30, v240\n"
    "v_lshl_add_u32 v195, v195, 2, v240\n"
    "s_add_u32 m0, s95, 35840\n"
    "s_nop 0\n"
    "buffer_load_dwordx4 v9, s[84:87], s91 offen lds\n"
    "s_lshl3_add_u32 s71, s74, 21233664\n"
    "v_lshl_add_u32 v232, v195, 7, s71\n"
    "v_lshlrev_b32 v194, 6, v241\n"
    "v_xor_b32 v246, v194, v243\n"
    "v_lshrrev_b32 v194, 1, v241\n"
    "v_lshlrev_b32 v194, 6, v194\n"
    "s_add_u32 m0, s98, 0\n"
    "s_nop 0\n"
    "buffer_load_dwordx4 v6, s[48:51], s89 offen lds\n"
    "v_xor_b32 v194, v194, v243\n"
    "s_lshl_b32 s71, s74, 12\n"
    "s_add_u32 s71, s71, 49152\n"
    "v_add_u32 v244, s71, v194\n"
    "v_xor_b32 v245, 128, v244\n"
    "s_lshl1_add_u32 s71, s70, s75\n"
    "s_add_u32 m0, s98, 1024\n"
    "s_nop 0\n"
    "buffer_load_dwordx4 v7, s[48:51], s89 offen lds\n"
    "s_lshr_b32 s72, s34, 1\n"
    "s_lshl1_add_u32 s71, s71, s72\n"
    "s_and_b32 s72, s34, 1\n"
    "s_lshl3_add_u32 s71, s71, s72\n"
    "s_lshl4_add_u32 s71, s71, s35\n"
    "s_xor_b32 s71, s71, 3\n"
    "buffer_load_dwordx4 v[12:15], v243, s[36:39], s78 offen\n"
    "buffer_load_dwordx4 v[16:19], v243, s[36:39], s78 offen offset:1024\n"
    "global_load_dwordx4 v[20:23], v243, s[24:25]\n"
    "s_and_b32 s94, s77, 1\n"
    "s_xor_b32 s94, s94, 1\n"
    "s_lshl_b32 s88, s94, 7\n"
    "s_mul_i32 s89, s94, 229376\n"
    "s_mul_i32 s90, s94, s92\n"
    "s_add_u32 s91, s90, s93\n"
    "s_add_u32 m0, s95, 53248\n"
    "s_nop 0\n"
    "buffer_load_dwordx4 v0, s[36:39], s88 offen lds\n"
    "s_lshl1_add_u32 s71, s71, s76\n"
    "s_lshl_b32 s71, s71, 7\n"
    "s_add_u32 s71, s71, 0\n"
    "v_lshrrev_b32 v194, 5, v240\n"
    "v_bfe_u32 v195, v240, 3, 2\n"
    "v_lshl_add_u32 v194, v194, 3, v195\n"
    "s_add_u32 m0, s95, 54272\n"
    "s_nop 0\n"
    "buffer_load_dwordx4 v1, s[36:39], s88 offen lds\n"
    "v_and_b32 v195, 7, v240\n"
    "v_lshl_add_u32 v194, v194, 6, v195\n"
    "v_lshl_add_u32 v234, v194, 4, s71\n"
    "v_lshrrev_b32 v194, 3, v240\n"
    "v_or_b32 v194, 0, v194\n"
    "v_and_b32 v196, 3, v240\n"
    "s_add_u32 m0, s95, 55296\n"
    "s_nop 0\n"
    "buffer_load_dwordx4 v2, s[36:39], s88 offen lds\n"
    "v_lshlrev_b32 v196, 2, v196\n"
    "v_xor_b32 v196, v196, v194\n"
    "v_lshl_add_u32 v196, v195, 5, v196\n"
    "s_lshl_b32 s71, s74, 12\n"
    "s_add_u32 s71, s71, 49152\n"
    "v_lshl_add_u32 v225, v196, 4, s71\n"
    "s_add_u32 m0, s95, 56320\n"
    "s_nop 0\n"
    "buffer_load_dwordx4 v3, s[36:39], s88 offen lds\n"
    "v_and_b32 v196, 1, v240\n"
    "v_bfe_u32 v233, v240, 1, 1\n"
    "v_lshl_add_u32 v196, v196, 1, v233\n"
    "v_lshlrev_b32 v233, 2, v196\n"
    "v_xor_b32 v194, v194, v233\n"
    "v_and_b32 v195, 4, v195\n"
    "s_add_u32 m0, s96, 102400\n"
    "s_nop 0\n"
    "buffer_load_dwordx4 v4, s[40:43], s88 offen lds\n"
    "v_or_b32 v195, v195, v196\n"
    "v_lshl_add_u32 v194, v195, 4, v194\n"
    "s_lshl_b32 s71, s74, 11\n"
    "s_add_u32 s71, s71, 65536\n"
    "v_lshl_add_u32 v233, v194, 4, s71\n"
    "s_lshl1_add_u32 s72, s70, s76\n"
    "s_add_u32 m0, s99, 53248\n"
    "s_nop 0\n"
    "buffer_load_dwordx4 v8, s[80:83], s90 offen lds\n"
    "s_lshl_b32 s72, s72, 10\n"
    "s_add_u32 s72, s72, 22085632\n"
    "v_add_u32 v28, s72, v243\n"
    "s_lshl_b32 s71, s76, 8\n"
    "s_add_u32 s71, s71, 106496\n"
    "v_lshl_add_u32 v29, v240, 2, s71\n"
    "s_add_u32 m0, s99, 54272\n"
    "s_nop 0\n"
    "buffer_load_dwordx4 v9, s[80:83], s90 offen lds\n"
    "s_lshl_b32 s71, s34, 6\n"
    "v_xor_b32 v30, s71, v29\n"
    "s_add_u32 m0, s97, 53248\n"
    "s_nop 0\n"
    "buffer_load_dwordx4 v6, s[44:47], s89 offen lds\n"
    "s_waitcnt vmcnt(11)\n"
    "s_barrier\n"
    "s_add_u32 m0, s97, 54272\n"
    "s_nop 0\n"
    "buffer_load_dwordx4 v7, s[44:47], s89 offen lds\n"
    "s_add_u32 m0, s95, 88064\n"
    "s_nop 0\n"
    "buffer_load_dwordx4 v8, s[84:87], s91 offen lds\n"
    "s_add_u32 m0, s95, 89088\n"
    "s_nop 0\n"
    "buffer_load_dwordx4 v9, s[84:87], s91 offen lds\n"
    "s_add_u32 m0, s98, 53248\n"
    "s_nop 0\n"
    "buffer_load_dwordx4 v6, s[48:51], s89 offen lds\n"
    "s_add_u32 m0, s98, 54272\n"
    "s_nop 0\n"
    "buffer_load_dwordx4 v7, s[48:51], s89 offen lds\n"
    "s_and_b32 s94, s77, 1\n"
    "s_xor_b32 s94, s94, 2\n"
    "s_lshl_b32 s88, s94, 7\n"
    "s_mul_i32 s89, s94, 229376\n"
    "s_mul_i32 s90, s94, s92\n"
    "s_add_u32 s91, s90, s93\n"
    "s_add_u32 m0, s95, 106496\n"
    "s_nop 0\n"
    "buffer_load_dwordx4 v0, s[36:39], s88 offen lds\n"
    "s_add_u32 m0, s95, 107520\n"
    "s_nop 0\n"
    "buffer_load_dwordx4 v1, s[36:39], s88 offen lds\n"
    "s_add_u32 m0, s95, 108544\n"
    "s_nop 0\n"
    "buffer_load_dwordx4 v2, s[36:39], s88 offen lds\n"
    "s_add_u32 m0, s95, 109568\n"
    "s_nop 0\n"
    "buffer_load_dwordx4 v3, s[36:39], s88 offen lds\n"
    "s_add_u32 m0, s96, 155648\n"
    "s_nop 0\n"
    "buffer_load_dwordx4 v4, s[40:43], s88 offen lds\n"
    "s_add_u32 m0, s99, 106496\n"
    "s_nop 0\n"
    "buffer_load_dwordx4 v8, s[80:83], s90 offen lds\n"
    "s_add_u32 m0, s99, 107520\n"
    "s_nop 0\n"
    "buffer_load_dwordx4 v9, s[80:83], s90 offen lds\n"
    "s_add_u32 m0, s97, 106496\n"
    "s_nop 0\n"
    "buffer_load_dwordx4 v6, s[44:47], s89 offen lds\n"
    "s_add_u32 m0, s97, 107520\n"
    "s_nop 0\n"
    "buffer_load_dwordx4 v7, s[44:47], s89 offen lds\n"
    "s_add_u32 m0, s95, 141312\n"
    "s_nop 0\n"
    "buffer_load_dwordx4 v8, s[84:87], s91 offen lds\n"
    "s_add_u32 m0, s95, 142336\n"
    "s_nop 0\n"
    "buffer_load_dwordx4 v9, s[84:87], s91 offen lds\n"
    "s_add_u32 m0, s98, 106496\n"
    "s_nop 0\n"
    "buffer_load_dwordx4 v6, s[48:51], s89 offen lds\n"
    "s_add_u32 m0, s98, 107520\n"
    "s_nop 0\n"
    "buffer_load_dwordx4 v7, s[48:51], s89 offen lds\n"
    "s_waitcnt lgkmcnt(0)\n"
    "s_mul_hi_u32 s71, s68, 1284865837\n"
    "s_mul_i32 s72, s68, 1481765933\n"
    "s_add_u32 s71, s71, s72\n"
    "s_mul_i32 s72, s69, 1284865837\n"
    "s_add_u32 s71, s71, s72\n"
    "s_mul_i32 s68, s68, 1284865837\n"
    "s_add_u32 s68, s68, 4150755663\n"
    "s_addc_u32 s69, s71, 335903614\n"
    "v_mov_b32 v200, s68\n"
    "v_mov_b32 v201, s69\n"
    "s_waitcnt vmcnt(13)\n"
    "s_barrier\n"
    "s_and_b32 s94, s77, 1\n"
    "s_xor_b32 s94, s94, 3\n"
    "s_lshl_b32 s88, s94, 7\n"
    "s_mul_i32 s89, s94, 229376\n"
    "s_mul_i32 s90, s94, s92\n"
    "s_add_u32 s91, s90, s93\n"
    "s_add_u32 m0, s95, 0\n"
    "s_nop 0\n"
    "buffer_load_dwordx4 v0, s[36:39], s88 offen lds\n"
    "s_add_u32 m0, s95, 1024\n"
    "s_nop 0\n"
    "buffer_load_dwordx4 v1, s[36:39], s88 offen lds\n"
    "s_add_u32 m0, s95, 2048\n"
    "s_nop 0\n"
    "buffer_load_dwordx4 v2, s[36:39], s88 offen lds\n"
    "s_add_u32 m0, s95, 3072\n"
    "s_nop 0\n"
    "buffer_load_dwordx4 v3, s[36:39], s88 offen lds\n"
    "s_add_u32 m0, s96, 49152\n"
    "s_nop 0\n"
    "buffer_load_dwordx4 v4, s[40:43], s88 offen lds\n"
    "s_add_u32 m0, s99, 0\n"
    "s_nop 0\n"
    "buffer_load_dwordx4 v8, s[80:83], s90 offen lds\n"
    "s_add_u32 m0, s99, 1024\n"
    "s_nop 0\n"
    "buffer_load_dwordx4 v9, s[80:83], s90 offen lds\n"
    "s_add_u32 m0, s97, 0\n"
    "s_nop 0\n"
    "buffer_load_dwordx4 v6, s[44:47], s89 offen lds\n"
    "s_add_u32 m0, s97, 1024\n"
    "s_nop 0\n"
    "buffer_load_dwordx4 v7, s[44:47], s89 offen lds\n"
    "s_add_u32 m0, s95, 34816\n"
    "s_nop 0\n"
    "buffer_load_dwordx4 v8, s[84:87], s91 offen lds\n"
    "s_add_u32 m0, s95, 35840\n"
    "s_nop 0\n"
    "buffer_load_dwordx4 v9, s[84:87], s91 offen lds\n"
    "s_add_u32 m0, s98, 0\n"
    "s_nop 0\n"
    "buffer_load_dwordx4 v6, s[48:51], s89 offen lds\n"
    "s_add_u32 m0, s98, 1024\n"
    "s_nop 0\n"
    "buffer_load_dwordx4 v7, s[48:51], s89 offen lds\n"
    "v_mov_b64 v[24:25], 0\n"
    "s_lshl2_add_u32 s73, s70, s34\n"
    "s_lshl2_add_u32 s73, s73, s35\n"
    "s_lshl2_add_u32 s73, s73, s74\n"
    "s_lshl_b32 s73, s73, 5\n"
    "s_add_u32 s73, s73, 22085632\n"
    "v_dot2c_f32_bf16 v24, v12, v20\n"
    "v_dot2c_f32_bf16 v25, v16, v20\n"
    "v_dot2c_f32_bf16 v24, v13, v21\n"
    "v_dot2c_f32_bf16 v25, v17, v21\n"
    "v_dot2c_f32_bf16 v24, v14, v22\n"
    "v_dot2c_f32_bf16 v25, v18, v22\n"
    "v_dot2c_f32_bf16 v24, v15, v23\n"
    "v_dot2c_f32_bf16 v25, v19, v23\n"
    "v_lshrrev_b32 v26, 5, v240\n"
    "v_lshl_add_u32 v26, v26, 4, s73\n"
    "v_mov_b32 v13, s68\n"
    "v_mov_b32 v15, s68\n"
    "v_permlane32_swap_b32 v24, v25\n"
    "s_nop 1\n"
    "v_add_f32 v24, v24, v25\n"
    "s_nop 1\n"
    "v_add_f32_dpp v24, v24, v24 row_shr:1 row_mask:0xf bank_mask:0xf\n"
    "s_nop 1\n"
    "v_add_f32_dpp v24, v24, v24 row_shr:2 row_mask:0xf bank_mask:0xf\n"
    "s_nop 1\n"
    "v_add_f32_dpp v24, v24, v24 row_shr:4 row_mask:0xf bank_mask:0xf\n"
    "s_nop 1\n"
    "v_add_f32_dpp v24, v24, v24 row_shr:8 row_mask:0xf bank_mask:0xf\n"
    "s_nop 1\n"
    "v_add_f32_dpp v24, v24, v24 row_bcast:15 row_mask:0xa bank_mask:0xf\n"
    "v_mul_f32 v25, 0xbfb8aa3b, v24\n"
    "v_exp_f32_e64 v24, -v25\n"
    "v_exp_f32 v25, v25\n"
    "v_add_f32 v24, 1.0, v24\n"
    "v_add_f32 v25, 1.0, v25\n"
    "v_min_f32 v12, 0x7f7fffff, v24\n"
    "v_rcp_f32 v14, v25\n"
    "s_mov_b32 exec_lo, 0x80000000\n"
    "s_mov_b32 exec_hi, 0x80000000\n"
    "buffer_store_dwordx4 v[12:15], v26, s[60:63], 0 offen sc1\n"
    "s_mov_b64 exec, -1\n"
    "s_waitcnt vmcnt(14)\n"
    "s_barrier\n"
    "s_xor_b32 s94, s77, 4\n"
    "s_lshl_b32 s88, s94, 7\n"
    "s_mul_i32 s89, s94, 229376\n"
    "s_mul_i32 s90, s94, s92\n"
    "s_add_u32 s91, s90, s93\n"
    "s_add_u32 m0, s95, 53248\n"
    "s_nop 0\n"
    "buffer_load_dwordx4 v0, s[36:39], s88 offen lds\n"
    "s_add_u32 m0, s95, 54272\n"
    "s_nop 0\n"
    "buffer_load_dwordx4 v1, s[36:39], s88 offen lds\n"
    "s_add_u32 m0, s95, 55296\n"
    "s_nop 0\n"
    "buffer_load_dwordx4 v2, s[36:39], s88 offen lds\n"
    "s_add_u32 m0, s95, 56320\n"
    "s_nop 0\n"
    "buffer_load_dwordx4 v3, s[36:39], s88 offen lds\n"
    "s_add_u32 m0, s96, 102400\n"
    "s_nop 0\n"
    "buffer_load_dwordx4 v4, s[40:43], s88 offen lds\n"
    "s_add_u32 m0, s99, 53248\n"
    "s_nop 0\n"
    "buffer_load_dwordx4 v8, s[80:83], s90 offen lds\n"
    "s_add_u32 m0, s99, 54272\n"
    "s_nop 0\n"
    "buffer_load_dwordx4 v9, s[80:83], s90 offen lds\n"
    "s_add_u32 m0, s97, 53248\n"
    "s_nop 0\n"
    "buffer_load_dwordx4 v6, s[44:47], s89 offen lds\n"
    "s_add_u32 m0, s97, 54272\n"
    "s_nop 0\n"
    "buffer_load_dwordx4 v7, s[44:47], s89 offen lds\n"
    "s_add_u32 m0, s95, 88064\n"
    "s_nop 0\n"
    "buffer_load_dwordx4 v8, s[84:87], s91 offen lds\n"
    "s_add_u32 m0, s95, 89088\n"
    "s_nop 0\n"
    "buffer_load_dwordx4 v9, s[84:87], s91 offen lds\n"
    "s_add_u32 m0, s98, 53248\n"
    "s_nop 0\n"
    "buffer_load_dwordx4 v6, s[48:51], s89 offen lds\n"
    "s_add_u32 m0, s98, 54272\n"
    "s_nop 0\n"
    "buffer_load_dwordx4 v7, s[48:51], s89 offen lds\n"
    "s_waitcnt vmcnt(14)\n"
    "s_barrier\n"
    "s_xor_b32 s94, s77, 5\n"
    "s_lshl_b32 s88, s94, 7\n"
    "s_mul_i32 s89, s94, 229376\n"
    "s_mul_i32 s90, s94, s92\n"
    "s_add_u32 s91, s90, s93\n"
    "s_add_u32 m0, s95, 106496\n"
    "s_nop 0\n"
    "buffer_load_dwordx4 v0, s[36:39], s88 offen lds\n"
    "s_add_u32 m0, s95, 107520\n"
    "s_nop 0\n"
    "buffer_load_dwordx4 v1, s[36:39], s88 offen lds\n"
    "s_add_u32 m0, s95, 108544\n"
    "s_nop 0\n"
    "buffer_load_dwordx4 v2, s[36:39], s88 offen lds\n"
    "s_add_u32 m0, s95, 109568\n"
    "s_nop 0\n"
    "buffer_load_dwordx4 v3, s[36:39], s88 offen lds\n"
    "s_add_u32 m0, s96, 155648\n"
    "s_nop 0\n"
    "buffer_load_dwordx4 v4, s[40:43], s88 offen lds\n"
    "s_add_u32 m0, s99, 106496\n"
    "s_nop 0\n"
    "buffer_load_dwordx4 v8, s[80:83], s90 offen lds\n"
    "s_add_u32 m0, s99, 107520\n"
    "s_nop 0\n"
    "buffer_load_dwordx4 v9, s[80:83], s90 offen lds\n"
    "s_add_u32 m0, s97, 106496\n"
    "s_nop 0\n"
    "buffer_load_dwordx4 v6, s[44:47], s89 offen lds\n"
    "s_add_u32 m0, s97, 107520\n"
    "s_nop 0\n"
    "buffer_load_dwordx4 v7, s[44:47], s89 offen lds\n"
    "s_add_u32 m0, s95, 141312\n"
    "s_nop 0\n"
    "buffer_load_dwordx4 v8, s[84:87], s91 offen lds\n"
    "s_add_u32 m0, s95, 142336\n"
    "s_nop 0\n"
    "buffer_load_dwordx4 v9, s[84:87], s91 offen lds\n"
    "s_add_u32 m0, s98, 106496\n"
    "s_nop 0\n"
    "buffer_load_dwordx4 v6, s[48:51], s89 offen lds\n"
    "s_add_u32 m0, s98, 107520\n"
    "s_nop 0\n"
    "buffer_load_dwordx4 v7, s[48:51], s89 offen lds\n"
    "buffer_load_dwordx4 v[32:35], v28, s[60:63], 0 offen sc1\n"
    "s_waitcnt vmcnt(14)\n"
    "s_barrier\n"
    "s_xor_b32 s94, s77, 6\n"
    "s_lshl_b32 s88, s94, 7\n"
    "s_mul_i32 s89, s94, 229376\n"
    "s_mul_i32 s90, s94, s92\n"
    "s_add_u32 s91, s90, s93\n"
    "s_add_u32 m0, s95, 0\n"
    "s_nop 0\n"
    "buffer_load_dwordx4 v0, s[36:39], s88 offen lds\n"
    "s_add_u32 m0, s95, 1024\n"
    "s_nop 0\n"
    "buffer_load_dwordx4 v1, s[36:39], s88 offen lds\n"
    "s_add_u32 m0, s95, 2048\n"
    "s_nop 0\n"
    "buffer_load_dwordx4 v2, s[36:39], s88 offen lds\n"
    "s_add_u32 m0, s95, 3072\n"
    "s_nop 0\n"
    "buffer_load_dwordx4 v3, s[36:39], s88 offen lds\n"
    "s_add_u32 m0, s96, 49152\n"
    "s_nop 0\n"
    "buffer_load_dwordx4 v4, s[40:43], s88 offen lds\n"
    "s_add_u32 m0, s99, 0\n"
    "s_nop 0\n"
    "buffer_load_dwordx4 v8, s[80:83], s90 offen lds\n"
    "s_add_u32 m0, s99, 1024\n"
    "s_nop 0\n"
    "buffer_load_dwordx4 v9, s[80:83], s90 offen lds\n"
    "s_add_u32 m0, s97, 0\n"
    "s_nop 0\n"
    "buffer_load_dwordx4 v6, s[44:47], s89 offen lds\n"
    "s_add_u32 m0, s97, 1024\n"
    "s_nop 0\n"
    "buffer_load_dwordx4 v7, s[44:47], s89 offen lds\n"
    "s_add_u32 m0, s95, 34816\n"
    ".LPAD0_%=:\n"
    "s_cbranch_execz .LRET_%=\n"
    "s_nop 0\n"
    "s_nop 0\n"
    "buffer_load_dwordx4 v8, s[84:87], s91 offen lds\n"
    "s_add_u32 m0, s95, 35840\n"
    "s_nop 0\n"
    "buffer_load_dwordx4 v9, s[84:87], s91 offen lds\n"
    "s_add_u32 m0, s98, 0\n"
    "s_nop 0\n"
    "buffer_load_dwordx4 v6, s[48:51], s89 offen lds\n"
    "s_add_u32 m0, s98, 1024\n"
    "s_nop 0\n"
    "buffer_load_dwordx4 v7, s[48:51], s89 offen lds\n"
    "s_waitcnt vmcnt(14)\n"
    "s_barrier\n"
    "s_xor_b32 s94, s77, 7\n"
    "s_lshl_b32 s88, s94, 7\n"
    "s_mul_i32 s89, s94, 229376\n"
    "s_mul_i32 s90, s94, s92\n"
    "s_add_u32 s91, s90, s93\n"
    "s_add_u32 m0, s95, 53248\n"
    "s_nop 0\n"
    "buffer_load_dwordx4 v0, s[36:39], s88 offen lds\n"
    "s_add_u32 m0, s95, 54272\n"
    "s_nop 0\n"
    "buffer_load_dwordx4 v1, s[36:39], s88 offen lds\n"
    "s_add_u32 m0, s95, 55296\n"
    "s_nop 0\n"
    "buffer_load_dwordx4 v2, s[36:39], s88 offen lds\n"
    "s_add_u32 m0, s95, 56320\n"
    "s_nop 0\n"
    "buffer_load_dwordx4 v3, s[36:39], s88 offen lds\n"
    "s_add_u32 m0, s96, 102400\n"
    "s_nop 0\n"
    "buffer_load_dwordx4 v4, s[40:43], s88 offen lds\n"
    "s_add_u32 m0, s99, 53248\n"
    "s_nop 0\n"
    "buffer_load_dwordx4 v8, s[80:83], s90 offen lds\n"
    "s_add_u32 m0, s99, 54272\n"
    "s_nop 0\n"
    "buffer_load_dwordx4 v9, s[80:83], s90 offen lds\n"
    "s_add_u32 m0, s95, 88064\n"
    "s_nop 0\n"
    "buffer_load_dwordx4 v8, s[84:87], s91 offen lds\n"
    "s_add_u32 m0, s95, 89088\n"
    "s_nop 0\n"
    "buffer_load_dwordx4 v9, s[84:87], s91 offen lds\n"
    "s_add_u32 m0, s97, 53248\n"
    "s_nop 0\n"
    "buffer_load_dwordx4 v6, s[44:47], s89 offen lds\n"
    "s_add_u32 m0, s97, 54272\n"
    "s_nop 0\n"
    "buffer_load_dwordx4 v7, s[44:47], s89 offen lds\n"
    "s_add_u32 m0, s98, 53248\n"
    "s_nop 0\n"
    "buffer_load_dwordx4 v6, s[48:51], s89 offen lds\n"
    "s_add_u32 m0, s98, 54272\n"
    "s_nop 0\n"
    "buffer_load_dwordx4 v7, s[48:51], s89 offen lds\n"
    "s_cmp_eq_u32 s35, 3\n"
    "s_cbranch_scc1 .LTK1_%=\n"
    "buffer_load_dword v40, v36, s[52:55], 0 offen offset:0\n"
    ".LTK1_%=:\n"
    "s_waitcnt vmcnt(5)\n"
    "s_cmp_eq_u32 s35, 3\n"
    "s_cbranch_scc0 .LVW2_%=\n"
    "s_waitcnt vmcnt(4)\n"
    ".LVW2_%=:\n"
    "s_barrier\n"
    "s_mov_b32 s78, 131072\n"
    "s_branch .LBZC_%=\n"
    ".LBZL_%=:\n"
    "buffer_load_dwordx4 v[32:35], v28, s[60:63], 0 offen sc1\n"
    "s_waitcnt vmcnt(0)\n"
    ".LBZC_%=:\n"
    "v_cmp_ne_u32 s[72:73], s68, v33\n"
    "v_cmp_ne_u32 vcc, s68, v35\n"
    "s_or_b64 vcc, vcc, s[72:73]\n"
    "s_cbranch_scc0 .LBZK_%=\n"
    "s_sub_u32 s78, s78, 1\n"
    "s_cbranch_scc0 .LBZL_%=\n"
    ".LBZK_%=:\n"
    "s_and_b32 s71, s34, 2\n"
    "s_lshl_b32 s71, s71, 30\n"
    "v_xor_b32 v34, s71, v34\n"
    "ds_write_b32 v29, v32\n"
    "ds_write_b32 v30, v34 offset:512\n"
    "ds_write_b32 v29, v200 offset:1024\n"
    "ds_write_b32 v29, v201 offset:1536\n"
    "s_xor_b32 s77, s70, s35\n"
    "s_and_b32 s77, s77, 3\n"
    "s_lshl_b32 s91, s74, 12\n"
    "s_waitcnt lgkmcnt(0)\n"
    "s_waitcnt vmcnt(1)\n"
    "s_cmp_eq_u32 s35, 3\n"
    "s_cbranch_scc0 .LVW3_%=\n"
    "s_waitcnt vmcnt(0)\n"
    ".LVW3_%=:\n"
    "s_barrier\n"
    "s_mov_b64 exec, 0xff\n"
    "v_mov_b64 v[52:53], 0\n"
    "buffer_atomic_add_x2 v[52:53], v197, s[60:63], 0 offen sc1\n"
    "s_lshl2_add_u32 s71, s30, s74\n"
    "s_lshl_b32 s71, s71, 7\n"
    "s_add_u32 s71, s71, 21233664\n"
    "v_lshl_add_u32 v194, v240, 3, s71\n"
    "buffer_atomic_add_x2 v[52:53], v194, s[60:63], 0 offen sc1\n"
    "s_mov_b64 exec, -1\n"
    "s_mov_b64 exec, 0\n"
    "s_cmp_eq_u32 s74, 0\n"
    "s_cbranch_scc0 .LWb0_%=\n"
    "s_call_b64 s[18:19], .LPAD1_%=\n"
    "s_nop 0\n"
    ".LWb0_%=:\n"
    "s_cmp_eq_u32 s74, 1\n"
    "s_cbranch_scc0 .LWb1_%=\n"
    "s_call_b64 s[18:19], .LPAD2_%=\n"
    "s_nop 0\n"
    ".LWb1_%=:\n"
    "s_cmp_eq_u32 s74, 2\n"
    "s_cbranch_scc0 .LWb2_%=\n"
    "s_call_b64 s[18:19], .LPAD5_%=\n"
    "s_nop 0\n"
    ".LWb2_%=:\n"
    "s_cmp_eq_u32 s74, 3\n"
    "s_cbranch_scc0 .LWb3_%=\n"
    "s_call_b64 s[18:19], .LPAD6_%=\n"
    "s_nop 0\n"
    ".LWb3_%=:\n"
    "s_mov_b64 exec, -1\n"
    "ds_read_b32 v248, v235 offset:0\n"
    "ds_read_b32 v249, v235 offset:64\n"
    "ds_read_b32 v250, v235 offset:128\n"
    "ds_read_b32 v251, v235 offset:192\n"
    "s_xor_b32 s71, s30, 1\n"
    "s_mul_i32 s71, s71, 12\n"
    "s_add_u32 s71, s71, s74\n"
    "s_lshl_b32 s71, s71, 11\n"
    "s_add_u32 s8, s71, 12582912\n"
    "s_xor_b32 s71, s30, 2\n"
    "s_mul_i32 s71, s71, 12\n"
    "s_add_u32 s71, s71, s74\n"
    "s_lshl_b32 s71, s71, 11\n"
    "s_add_u32 s9, s71, 12591104\n"
    "s_xor_b32 s71, s30, 3\n"
    "s_mul_i32 s71, s71, 12\n"
    "s_add_u32 s71, s71, s74\n"
    "s_lshl_b32 s71, s71, 11\n"
    "s_add_u32 s10, s71, 12599296\n"
    "s_barrier\n"
    "s_cmp_eq_u32 s35, 3\n"
    "s_cbranch_scc1 .LTK4_%=\n"
    "buffer_load_dword v43, v38, s[52:55], 0 offen\n"
    ".LTK4_%=:\n"
    "buffer_load_dword v42, v37, s[56:59], 0 offen\n"
    "s_waitcnt lgkmcnt(0)\n"
    "s_barrier\n"
    "s_sleep 3\n"
    "s_mov_b32 s88, 0\n"
    "s_add_u32 m0, s91, 114688\n"
    "s_nop 0\n"
    "buffer_load_dwordx4 v204, s[52:55], s88 offen lds\n"
    "s_add_u32 m0, s91, 115712\n"
    "s_nop 0\n"
    "buffer_load_dwordx4 v205, s[52:55], s88 offen lds\n"
    "s_add_u32 m0, s91, 116736\n"
    "s_nop 0\n"
    "buffer_load_dwordx4 v206, s[52:55], s88 offen lds\n"
    "s_add_u32 m0, s91, 117760\n"
    "s_nop 0\n"
    "buffer_load_dwordx4 v207, s[52:55], s88 offen lds\n"
    "s_mov_b32 s88, 128\n"
    "s_add_u32 m0, s91, 131072\n"
    "s_nop 0\n"
    "buffer_load_dwordx4 v204, s[52:55], s88 offen lds\n"
    "s_add_u32 m0, s91, 132096\n"
    "s_nop 0\n"
    "buffer_load_dwordx4 v205, s[52:55], s88 offen lds\n"
    "s_add_u32 m0, s91, 133120\n"
    "s_nop 0\n"
    "buffer_load_dwordx4 v206, s[52:55], s88 offen lds\n"
    "s_add_u32 m0, s91, 134144\n"
    "s_nop 0\n"
    "buffer_load_dwordx4 v207, s[52:55], s88 offen lds\n"
    "s_mov_b32 s88, 256\n"
    "s_add_u32 m0, s91, 147456\n"
    "s_nop 0\n"
    "buffer_load_dwordx4 v204, s[52:55], s88 offen lds\n"
    "s_add_u32 m0, s91, 148480\n"
    "s_nop 0\n"
    "buffer_load_dwordx4 v205, s[52:55], s88 offen lds\n"
    "s_add_u32 m0, s91, 149504\n"
    "s_nop 0\n"
    "buffer_load_dwordx4 v206, s[52:55], s88 offen lds\n"
    "s_add_u32 m0, s91, 150528\n"
    "s_nop 0\n"
    "buffer_load_dwordx4 v207, s[52:55], s88 offen lds\n"
    "s_mov_b64 exec, 255\n"
    "v_mov_b64 v[50:51], 0\n"
    "buffer_atomic_add_x2 v[50:51], v197, s[60:63], 0 offen sc0 sc1\n"
    "s_mov_b64 exec, -1\n"
    "s_mov_b32 s88, 384\n"
    "s_add_u32 m0, s91, 98304\n"
    "s_nop 0\n"
    "buffer_load_dwordx4 v204, s[52:55], s88 offen lds\n"
    "s_add_u32 m0, s91, 99328\n"
    "s_nop 0\n"
    "buffer_load_dwordx4 v205, s[52:55], s88 offen lds\n"
    "s_add_u32 m0, s91, 100352\n"
    "s_nop 0\n"
    "buffer_load_dwordx4 v206, s[52:55], s88 offen lds\n"
    "s_add_u32 m0, s91, 101376\n"
    "s_nop 0\n"
    "buffer_load_dwordx4 v207, s[52:55], s88 offen lds\n"
    "s_mov_b32 s89, 0\n"
    "s_mov_b64 exec, 255\n"
    "s_mov_b32 s78, 131072\n"
    "v_mov_b64 v[44:45], 0\n"
    "buffer_atomic_add_x2 v[44:45], v197, s[60:63], 0 offen sc0 sc1\n"
    "s_waitcnt vmcnt(5)\n"
    "v_cmp_ne_u64_e32 vcc, s[68:69], v[50:51]\n"
    "s_cbranch_vccz .LBAK_%=\n"
    ".LBAL_%=:\n"
    "s_sleep 2\n"
    "v_mov_b64 v[46:47], 0\n"
    "buffer_atomic_add_x2 v[46:47], v197, s[60:63], 0 offen sc0 sc1\n"
    "s_waitcnt vmcnt(1)\n"
    "v_cmp_ne_u64_e32 vcc, s[68:69], v[44:45]\n"
    "s_cbranch_vccz .LBAK_%=\n"
    "s_sleep 2\n"
    "v_mov_b64 v[44:45], 0\n"
    "buffer_atomic_add_x2 v[44:45], v197, s[60:63], 0 offen sc0 sc1\n"
    "s_waitcnt vmcnt(1)\n"
    "v_cmp_ne_u64_e32 vcc, s[68:69], v[46:47]\n"
    "s_cbranch_vccz .LBAK_%=\n"
    "s_sub_u32 s78, s78, 1\n"
    "s_cbranch_scc0 .LBAL_%=\n"
    ".LBAK_%=:\n"
    "s_mov_b64 exec, -1\n"
    "s_add_u32 m0, s91, 0\n"
    "s_nop 0\n"
    "buffer_load_dwordx4 v208, s[60:63], s89 offen offset:0 sc1 lds\n"
    "buffer_load_dwordx4 v208, s[60:63], s89 offen offset:1024 sc1 lds\n"
    "buffer_load_dwordx4 v208, s[60:63], s89 offen offset:2048 sc1 lds\n"
    "buffer_load_dwordx4 v208, s[60:63], s89 offen offset:3072 sc1 lds\n"
    "s_mov_b32 s89, 65536\n"
    "s_add_u32 m0, s91, 16384\n"
    "s_nop 0\n"
    "buffer_load_dwordx4 v208, s[60:63], s89 offen offset:0 sc1 lds\n"
    "buffer_load_dwordx4 v208, s[60:63], s89 offen offset:1024 sc1 lds\n"
    "buffer_load_dwordx4 v208, s[60:63], s89 offen offset:2048 sc1 lds\n"
    "buffer_load_dwordx4 v208, s[60:63], s89 offen offset:3072 sc1 lds\n"
    "s_mov_b32 s89, 131072\n"
    "s_add_u32 m0, s91, 32768\n"
    "s_nop 0\n"
    "buffer_load_dwordx4 v208, s[60:63], s89 offen offset:0 sc1 lds\n"
    "buffer_load_dwordx4 v208, s[60:63], s89 offen offset:1024 sc1 lds\n"
    "buffer_load_dwordx4 v208, s[60:63], s89 offen offset:2048 sc1 lds\n"
    "buffer_load_dwordx4 v208, s[60:63], s89 offen offset:3072 sc1 lds\n"
    "s_setprio 0\n"
    "s_waitcnt vmcnt(8)\n"
    "s_barrier\n"
    "ds_read_b128 v[96:99], v222 offset:0\n"
    "ds_read_b128 v[100:103], v223 offset:0\n"
    "ds_read_b128 v[64:67], v224 offset:0\n"
    "ds_read_b128 v[68:71], v224 offset:1024\n"
    "ds_read_b128 v[72:75], v224 offset:2048\n"
    "ds_read_b128 v[76:79], v224 offset:3072\n"
    "ds_read_b128 v[80:83], v224 offset:4096\n"
    "ds_read_b128 v[84:87], v224 offset:5120\n"
    "ds_read_b128 v[88:91], v224 offset:6144\n"
    "ds_read_b128 v[92:95], v224 offset:7168\n"
    "ds_read_b128 v[104:107], v222 offset:2048\n"
    "ds_read_b128 v[108:111], v223 offset:2048\n"
    "ds_read_b128 v[112:115], v222 offset:4096\n"
    "ds_read_b128 v[116:119], v223 offset:4096\n"
    "ds_read_b128 v[120:123], v222 offset:6144\n"
    "ds_read_b128 v[124:127], v223 offset:6144\n"
    "s_waitcnt vmcnt(4) lgkmcnt(0)\n"
    "s_barrier\n"
    "v_mfma_f32_16x16x32_bf16 v[0:3], v[64:67], v[96:99], 0\n"
    "ds_read_b128 v[160:163], v222 offset:16384\n"
    "v_mfma_f32_16x16x32_bf16 v[0:3], v[68:71], v[100:103], v[0:3]\n"
    "ds_read_b128 v[164:167], v223 offset:16384\n"
    "v_mfma_f32_16x16x32_bf16 v[4:7], v[72:75], v[96:99], 0\n"
    "ds_read_b128 v[128:131], v224 offset:16384\n"
    "v_mfma_f32_16x16x32_bf16 v[4:7], v[76:79], v[100:103], v[4:7]\n"
    "ds_read_b128 v[132:135], v224 offset:17408\n"
    "v_mfma_f32_16x16x32_bf16 v[8:11], v[80:83], v[96:99], 0\n"
    "ds_read_b128 v[136:139], v224 offset:18432\n"
    "v_mfma_f32_16x16x32_bf16 v[8:11], v[84:87], v[100:103], v[8:11]\n"
    "ds_read_b128 v[140:143], v224 offset:19456\n"
    "v_mfma_f32_16x16x32_bf16 v[12:15], v[88:91], v[96:99], 0\n"
    "ds_read_b128 v[144:147], v224 offset:20480\n"
    "v_mfma_f32_16x16x32_bf16 v[12:15], v[92:95], v[100:103], v[12:15]\n"
    "ds_read_b128 v[148:151], v224 offset:21504\n"
    "v_mfma_f32_16x16x32_bf16 v[28:31], v[88:91], v[104:107], 0\n"
    "ds_read_b128 v[152:155], v224 offset:22528\n"
    "v_mfma_f32_16x16x32_bf16 v[28:31], v[92:95], v[108:111], v[28:31]\n"
    "ds_read_b128 v[156:159], v224 offset:23552\n"
    "v_mfma_f32_16x16x32_bf16 v[24:27], v[80:83], v[104:107], 0\n"
    "ds_read_b128 v[168:171], v222 offset:18432\n"
    "v_mfma_f32_16x16x32_bf16 v[24:27], v[84:87], v[108:111], v[24:27]\n"
    "ds_read_b128 v[172:175], v223 offset:18432\n"
    "v_mfma_f32_16x16x32_bf16 v[20:23], v[72:75], v[104:107], 0\n"
    "ds_read_b128 v[176:179], v222 offset:20480\n"
    "v_mfma_f32_16x16x32_bf16 v[20:23], v[76:79], v[108:111], v[20:23]\n"
    "ds_read_b128 v[180:183], v223 offset:20480\n"
    "v_mfma_f32_16x16x32_bf16 v[16:19], v[64:67], v[104:107], 0\n"
    "ds_read_b128 v[184:187], v222 offset:22528\n"
    "v_mfma_f32_16x16x32_bf16 v[16:19], v[68:71], v[108:111], v[16:19]\n"
    "ds_read_b128 v[188:191], v223 offset:22528\n"
    "v_mfma_f32_16x16x32_bf16 v[32:35], v[64:67], v[112:115], 0\n"
    "v_mfma_f32_16x16x32_bf16 v[32:35], v[68:71], v[116:119], v[32:35]\n"
    "v_mfma_f32_16x16x32_bf16 v[36:39], v[72:75], v[112:115], 0\n"
    "v_mfma_f32_16x16x32_bf16 v[36:39], v[76:79], v[116:119], v[36:39]\n"
    "v_mfma_f32_16x16x32_bf16 v[40:43], v[80:83], v[112:115], 0\n"
    "v_mfma_f32_16x16x32_bf16 v[40:43], v[84:87], v[116:119], v[40:43]\n"
    "v_mfma_f32_16x16x32_bf16 v[44:47], v[88:91], v[112:115], 0\n"
    "v_mfma_f32_16x16x32_bf16 v[44:47], v[92:95], v[116:119], v[44:47]\n"
    "v_mfma_f32_16x16x32_bf16 v[60:63], v[88:91], v[120:123], 0\n"
    "v_mfma_f32_16x16x32_bf16 v[60:63], v[92:95], v[124:127], v[60:63]\n"
    "v_mfma_f32_16x16x32_bf16 v[56:59], v[80:83], v[120:123], 0\n"
    "v_mfma_f32_16x16x32_bf16 v[56:59], v[84:87], v[124:127], v[56:59]\n"
    "v_mfma_f32_16x16x32_bf16 v[52:55], v[72:75], v[120:123], 0\n"
    "v_mfma_f32_16x16x32_bf16 v[52:55], v[76:79], v[124:127], v[52:55]\n"
    "v_mfma_f32_16x16x32_bf16 v[48:51], v[64:67], v[120:123], 0\n"
    "v_mfma_f32_16x16x32_bf16 v[48:51], v[68:71], v[124:127], v[48:51]\n"
    "s_waitcnt vmcnt(0) lgkmcnt(0)\n"
    "s_barrier\n"
    "v_mfma_f32_16x16x32_bf16 v[0:3], v[128:131], v[160:163], v[0:3]\n"
    "ds_read_b128 v[96:99], v222 offset:32768\n"
    "v_mfma_f32_16x16x32_bf16 v[0:3], v[132:135], v[164:167], v[0:3]\n"
    "ds_read_b128 v[100:103], v223 offset:32768\n"
    "v_mfma_f32_16x16x32_bf16 v[4:7], v[136:139], v[160:163], v[4:7]\n"
    "ds_read_b128 v[64:67], v224 offset:32768\n"
    "v_mfma_f32_16x16x32_bf16 v[4:7], v[140:143], v[164:167], v[4:7]\n"
    "ds_read_b128 v[68:71], v224 offset:33792\n"
    "v_mfma_f32_16x16x32_bf16 v[8:11], v[144:147], v[160:163], v[8:11]\n"
    "ds_read_b128 v[72:75], v224 offset:34816\n"
    "v_mfma_f32_16x16x32_bf16 v[8:11], v[148:151], v[164:167], v[8:11]\n"
    "ds_read_b128 v[76:79], v224 offset:35840\n"
    "v_mfma_f32_16x16x32_bf16 v[12:15], v[152:155], v[160:163], v[12:15]\n"
    "ds_read_b128 v[80:83], v224 offset:36864\n"
    "v_mfma_f32_16x16x32_bf16 v[12:15], v[156:159], v[164:167], v[12:15]\n"
    "ds_read_b128 v[84:87], v224 offset:37888\n"
    "v_mfma_f32_16x16x32_bf16 v[28:31], v[152:155], v[168:171], v[28:31]\n"
    "ds_read_b128 v[88:91], v224 offset:38912\n"
    "v_mfma_f32_16x16x32_bf16 v[28:31], v[156:159], v[172:175], v[28:31]\n"
    "ds_read_b128 v[92:95], v224 offset:39936\n"
    "v_mfma_f32_16x16x32_bf16 v[24:27], v[144:147], v[168:171], v[24:27]\n"
    "ds_read_b128 v[104:107], v222 offset:34816\n"
    "v_mfma_f32_16x16x32_bf16 v[24:27], v[148:151], v[172:175], v[24:27]\n"
    "ds_read_b128 v[108:111], v223 offset:34816\n"
    "v_mfma_f32_16x16x32_bf16 v[20:23], v[136:139], v[168:171], v[20:23]\n"
    "ds_read_b128 v[112:115], v222 offset:36864\n"
    "v_mfma_f32_16x16x32_bf16 v[20:23], v[140:143], v[172:175], v[20:23]\n"
    "ds_read_b128 v[116:119], v223 offset:36864\n"
    "v_mfma_f32_16x16x32_bf16 v[16:19], v[128:131], v[168:171], v[16:19]\n"
    "ds_read_b128 v[120:123], v222 offset:38912\n"
    "v_mfma_f32_16x16x32_bf16 v[16:19], v[132:135], v[172:175], v[16:19]\n"
    "ds_read_b128 v[124:127], v223 offset:38912\n"
    "v_mfma_f32_16x16x32_bf16 v[32:35], v[128:131], v[176:179], v[32:35]\n"
    "v_mfma_f32_16x16x32_bf16 v[32:35], v[132:135], v[180:183], v[32:35]\n"
    "v_mfma_f32_16x16x32_bf16 v[36:39], v[136:139], v[176:179], v[36:39]\n"
    "v_mfma_f32_16x16x32_bf16 v[36:39], v[140:143], v[180:183], v[36:39]\n"
    "v_mfma_f32_16x16x32_bf16 v[40:43], v[144:147], v[176:179], v[40:43]\n"
    "v_mfma_f32_16x16x32_bf16 v[40:43], v[148:151], v[180:183], v[40:43]\n"
    "v_mfma_f32_16x16x32_bf16 v[44:47], v[152:155], v[176:179], v[44:47]\n"
    "v_mfma_f32_16x16x32_bf16 v[44:47], v[156:159], v[180:183], v[44:47]\n"
    "v_mfma_f32_16x16x32_bf16 v[60:63], v[152:155], v[184:187], v[60:63]\n"
    "v_mfma_f32_16x16x32_bf16 v[60:63], v[156:159], v[188:191], v[60:63]\n"
    "v_mfma_f32_16x16x32_bf16 v[56:59], v[144:147], v[184:187], v[56:59]\n"
    "v_mfma_f32_16x16x32_bf16 v[56:59], v[148:151], v[188:191], v[56:59]\n"
    "v_mfma_f32_16x16x32_bf16 v[52:55], v[136:139], v[184:187], v[52:55]\n"
    "v_mfma_f32_16x16x32_bf16 v[52:55], v[140:143], v[188:191], v[52:55]\n"
    "v_mfma_f32_16x16x32_bf16 v[48:51], v[128:131], v[184:187], v[48:51]\n"
    "v_mfma_f32_16x16x32_bf16 v[48:51], v[132:135], v[188:191], v[48:51]\n"
    "s_waitcnt lgkmcnt(0)\n"
    "s_barrier\n"
    "v_mfma_f32_16x16x32_bf16 v[0:3], v[64:67], v[96:99], v[0:3]\n"
    "ds_read_b128 v[160:163], v220 offset:32768\n"
    "v_mfma_f32_16x16x32_bf16 v[0:3], v[68:71], v[100:103], v[0:3]\n"
    "ds_read_b128 v[164:167], v221 offset:32768\n"
    "v_mfma_f32_16x16x32_bf16 v[4:7], v[72:75], v[96:99], v[4:7]\n"
    "ds_read_b128 v[128:131], v224 offset:49152\n"
    "v_mfma_f32_16x16x32_bf16 v[4:7], v[76:79], v[100:103], v[4:7]\n"
    "ds_read_b128 v[132:135], v224 offset:50176\n"
    "v_mfma_f32_16x16x32_bf16 v[8:11], v[80:83], v[96:99], v[8:11]\n"
    "ds_read_b128 v[136:139], v224 offset:51200\n"
    "v_mfma_f32_16x16x32_bf16 v[8:11], v[84:87], v[100:103], v[8:11]\n"
    "ds_read_b128 v[140:143], v224 offset:52224\n"
    "v_mfma_f32_16x16x32_bf16 v[12:15], v[88:91], v[96:99], v[12:15]\n"
    "ds_read_b128 v[144:147], v224 offset:53248\n"
    "v_mfma_f32_16x16x32_bf16 v[12:15], v[92:95], v[100:103], v[12:15]\n"
    "ds_read_b128 v[148:151], v224 offset:54272\n"
    "v_mfma_f32_16x16x32_bf16 v[28:31], v[88:91], v[104:107], v[28:31]\n"
    "ds_read_b128 v[152:155], v224 offset:55296\n"
    "v_mfma_f32_16x16x32_bf16 v[28:31], v[92:95], v[108:111], v[28:31]\n"
    "ds_read_b128 v[156:159], v224 offset:56320\n"
    "v_mfma_f32_16x16x32_bf16 v[24:27], v[80:83], v[104:107], v[24:27]\n"
    "ds_read_b128 v[168:171], v220 offset:34816\n"
    "v_mfma_f32_16x16x32_bf16 v[24:27], v[84:87], v[108:111], v[24:27]\n"
    "ds_read_b128 v[172:175], v221 offset:34816\n"
    "v_mfma_f32_16x16x32_bf16 v[20:23], v[72:75], v[104:107], v[20:23]\n"
    "ds_read_b128 v[176:179], v220 offset:36864\n"
    "v_mfma_f32_16x16x32_bf16 v[20:23], v[76:79], v[108:111], v[20:23]\n"
    "ds_read_b128 v[180:183], v221 offset:36864\n"
    "v_mfma_f32_16x16x32_bf16 v[16:19], v[64:67], v[104:107], v[16:19]\n"
    "ds_read_b128 v[184:187], v220 offset:38912\n"
    "v_mfma_f32_16x16x32_bf16 v[16:19], v[68:71], v[108:111], v[16:19]\n"
    "ds_read_b128 v[188:191], v221 offset:38912\n"
    "v_mfma_f32_16x16x32_bf16 v[32:35], v[64:67], v[112:115], v[32:35]\n"
    "v_mfma_f32_16x16x32_bf16 v[32:35], v[68:71], v[116:119], v[32:35]\n"
    "v_mfma_f32_16x16x32_bf16 v[36:39], v[72:75], v[112:115], v[36:39]\n"
    "v_mfma_f32_16x16x32_bf16 v[36:39], v[76:79], v[116:119], v[36:39]\n"
    "v_mfma_f32_16x16x32_bf16 v[40:43], v[80:83], v[112:115], v[40:43]\n"
    "v_mfma_f32_16x16x32_bf16 v[40:43], v[84:87], v[116:119], v[40:43]\n"
    "v_mfma_f32_16x16x32_bf16 v[44:47], v[88:91], v[112:115], v[44:47]\n"
    "v_mfma_f32_16x16x32_bf16 v[44:47], v[92:95], v[116:119], v[44:47]\n"
    "v_mfma_f32_16x16x32_bf16 v[60:63], v[88:91], v[120:123], v[60:63]\n"
    "v_mfma_f32_16x16x32_bf16 v[60:63], v[92:95], v[124:127], v[60:63]\n"
    "v_mfma_f32_16x16x32_bf16 v[56:59], v[80:83], v[120:123], v[56:59]\n"
    "v_mfma_f32_16x16x32_bf16 v[56:59], v[84:87], v[124:127], v[56:59]\n"
    "v_mfma_f32_16x16x32_bf16 v[52:55], v[72:75], v[120:123], v[52:55]\n"
    "v_mfma_f32_16x16x32_bf16 v[52:55], v[76:79], v[124:127], v[52:55]\n"
    "v_mfma_f32_16x16x32_bf16 v[48:51], v[64:67], v[120:123], v[48:51]\n"
    "v_mfma_f32_16x16x32_bf16 v[48:51], v[68:71], v[124:127], v[48:51]\n"
    "s_waitcnt lgkmcnt(0)\n"
    "s_barrier\n"
    "v_mfma_f32_16x16x32_bf16 v[0:3], v[128:131], v[160:163], v[0:3]\n"
    "ds_read_b128 v[96:99], v222 offset:0\n"
    "v_mfma_f32_16x16x32_bf16 v[0:3], v[132:135], v[164:167], v[0:3]\n"
    "ds_read_b128 v[100:103], v223 offset:0\n"
    "v_mfma_f32_16x16x32_bf16 v[4:7], v[136:139], v[160:163], v[4:7]\n"
    "ds_read_b128 v[64:67], v224 offset:0\n"
    "v_mfma_f32_16x16x32_bf16 v[4:7], v[140:143], v[164:167], v[4:7]\n"
    "ds_read_b128 v[68:71], v224 offset:1024\n"
    "v_mfma_f32_16x16x32_bf16 v[8:11], v[144:147], v[160:163], v[8:11]\n"
    "ds_read_b128 v[72:75], v224 offset:2048\n"
    "v_mfma_f32_16x16x32_bf16 v[8:11], v[148:151], v[164:167], v[8:11]\n"
    "ds_read_b128 v[76:79], v224 offset:3072\n"
    "v_mfma_f32_16x16x32_bf16 v[12:15], v[152:155], v[160:163], v[12:15]\n"
    "ds_read_b128 v[80:83], v224 offset:4096\n"
    "v_mfma_f32_16x16x32_bf16 v[12:15], v[156:159], v[164:167], v[12:15]\n"
    "ds_read_b128 v[84:87], v224 offset:5120\n"
    "v_mfma_f32_16x16x32_bf16 v[28:31], v[152:155], v[168:171], v[28:31]\n"
    "ds_read_b128 v[88:91], v224 offset:6144\n"
    "v_mfma_f32_16x16x32_bf16 v[28:31], v[156:159], v[172:175], v[28:31]\n"
    "ds_read_b128 v[92:95], v224 offset:7168\n"
    "v_mfma_f32_16x16x32_bf16 v[24:27], v[144:147], v[168:171], v[24:27]\n"
    "ds_read_b128 v[104:107], v222 offset:2048\n"
    "v_mfma_f32_16x16x32_bf16 v[24:27], v[148:151], v[172:175], v[24:27]\n"
    "ds_read_b128 v[108:111], v223 offset:2048\n"
    "v_mfma_f32_16x16x32_bf16 v[20:23], v[136:139], v[168:171], v[20:23]\n"
    "ds_read_b128 v[112:115], v222 offset:4096\n"
    "v_mfma_f32_16x16x32_bf16 v[20:23], v[140:143], v[172:175], v[20:23]\n"
    "ds_read_b128 v[116:119], v223 offset:4096\n"
    "v_mfma_f32_16x16x32_bf16 v[16:19], v[128:131], v[168:171], v[16:19]\n"
    "ds_read_b128 v[120:123], v222 offset:6144\n"
    "v_mfma_f32_16x16x32_bf16 v[16:19], v[132:135], v[172:175], v[16:19]\n"
    "ds_read_b128 v[124:127], v223 offset:6144\n"
    "v_mfma_f32_16x16x32_bf16 v[32:35], v[128:131], v[176:179], v[32:35]\n"
    "v_mfma_f32_16x16x32_bf16 v[32:35], v[132:135], v[180:183], v[32:35]\n"
    "v_mfma_f32_16x16x32_bf16 v[36:39], v[136:139], v[176:179], v[36:39]\n"
    "v_mfma_f32_16x16x32_bf16 v[36:39], v[140:143], v[180:183], v[36:39]\n"
    "v_mfma_f32_16x16x32_bf16 v[40:43], v[144:147], v[176:179], v[40:43]\n"
    "v_mfma_f32_16x16x32_bf16 v[40:43], v[148:151], v[180:183], v[40:43]\n"
    "v_mfma_f32_16x16x32_bf16 v[44:47], v[152:155], v[176:179], v[44:47]\n"
    "v_mfma_f32_16x16x32_bf16 v[44:47], v[156:159], v[180:183], v[44:47]\n"
    "v_mfma_f32_16x16x32_bf16 v[60:63], v[152:155], v[184:187], v[60:63]\n"
    "v_mfma_f32_16x16x32_bf16 v[60:63], v[156:159], v[188:191], v[60:63]\n"
    "v_mfma_f32_16x16x32_bf16 v[56:59], v[144:147], v[184:187], v[56:59]\n"
    "v_mfma_f32_16x16x32_bf16 v[56:59], v[148:151], v[188:191], v[56:59]\n"
    "v_mfma_f32_16x16x32_bf16 v[52:55], v[136:139], v[184:187], v[52:55]\n"
    "v_mfma_f32_16x16x32_bf16 v[52:55], v[140:143], v[188:191], v[52:55]\n"
    "v_mfma_f32_16x16x32_bf16 v[48:51], v[128:131], v[184:187], v[48:51]\n"
    "v_mfma_f32_16x16x32_bf16 v[48:51], v[132:135], v[188:191], v[48:51]\n"
    "s_waitcnt lgkmcnt(0)\n"
    "s_barrier\n"
    "v_mfma_f32_16x16x32_bf16 v[0:3], v[64:67], v[96:99], v[0:3]\n"
    "ds_read_b128 v[160:163], v222 offset:16384\n"
    "v_mfma_f32_16x16x32_bf16 v[0:3], v[68:71], v[100:103], v[0:3]\n"
    "ds_read_b128 v[164:167], v223 offset:16384\n"
    "v_mfma_f32_16x16x32_bf16 v[4:7], v[72:75], v[96:99], v[4:7]\n"
    "ds_read_b128 v[128:131], v224 offset:16384\n"
    "v_mfma_f32_16x16x32_bf16 v[4:7], v[76:79], v[100:103], v[4:7]\n"
    "ds_read_b128 v[132:135], v224 offset:17408\n"
    "v_mfma_f32_16x16x32_bf16 v[8:11], v[80:83], v[96:99], v[8:11]\n"
    "ds_read_b128 v[136:139], v224 offset:18432\n"
    "v_mfma_f32_16x16x32_bf16 v[8:11], v[84:87], v[100:103], v[8:11]\n"
    "ds_read_b128 v[140:143], v224 offset:19456\n"
    "v_mfma_f32_16x16x32_bf16 v[12:15], v[88:91], v[96:99], v[12:15]\n"
    "ds_read_b128 v[144:147], v224 offset:20480\n"
    "v_mfma_f32_16x16x32_bf16 v[12:15], v[92:95], v[100:103], v[12:15]\n"
    "ds_read_b128 v[148:151], v224 offset:21504\n"
    "v_mfma_f32_16x16x32_bf16 v[28:31], v[88:91], v[104:107], v[28:31]\n"
    "ds_read_b128 v[152:155], v224 offset:22528\n"
    "v_mfma_f32_16x16x32_bf16 v[28:31], v[92:95], v[108:111], v[28:31]\n"
    "ds_read_b128 v[156:159], v224 offset:23552\n"
    "v_mfma_f32_16x16x32_bf16 v[24:27], v[80:83], v[104:107], v[24:27]\n"
    "ds_read_b128 v[168:171], v222 offset:18432\n"
    "v_mfma_f32_16x16x32_bf16 v[24:27], v[84:87], v[108:111], v[24:27]\n"
    "ds_read_b128 v[172:175], v223 offset:18432\n"
    "v_mfma_f32_16x16x32_bf16 v[20:23], v[72:75], v[104:107], v[20:23]\n"
    "ds_read_b128 v[176:179], v222 offset:20480\n"
    "v_mfma_f32_16x16x32_bf16 v[20:23], v[76:79], v[108:111], v[20:23]\n"
    "ds_read_b128 v[180:183], v223 offset:20480\n"
    "v_mfma_f32_16x16x32_bf16 v[16:19], v[64:67], v[104:107], v[16:19]\n"
    "ds_read_b128 v[184:187], v222 offset:22528\n"
    "v_mfma_f32_16x16x32_bf16 v[16:19], v[68:71], v[108:111], v[16:19]\n"
    "ds_read_b128 v[188:191], v223 offset:22528\n"
    "v_mfma_f32_16x16x32_bf16 v[32:35], v[64:67], v[112:115], v[32:35]\n"
    "v_mfma_f32_16x16x32_bf16 v[32:35], v[68:71], v[116:119], v[32:35]\n"
    "v_mfma_f32_16x16x32_bf16 v[36:39], v[72:75], v[112:115], v[36:39]\n"
    "v_mfma_f32_16x16x32_bf16 v[36:39], v[76:79], v[116:119], v[36:39]\n"
    "v_mfma_f32_16x16x32_bf16 v[40:43], v[80:83], v[112:115], v[40:43]\n"
    "v_mfma_f32_16x16x32_bf16 v[40:43], v[84:87], v[116:119], v[40:43]\n"
    "v_mfma_f32_16x16x32_bf16 v[44:47], v[88:91], v[112:115], v[44:47]\n"
    "v_mfma_f32_16x16x32_bf16 v[44:47], v[92:95], v[116:119], v[44:47]\n"
    "v_mfma_f32_16x16x32_bf16 v[60:63], v[88:91], v[120:123], v[60:63]\n"
    "v_mfma_f32_16x16x32_bf16 v[60:63], v[92:95], v[124:127], v[60:63]\n"
    "v_mfma_f32_16x16x32_bf16 v[56:59], v[80:83], v[120:123], v[56:59]\n"
    "v_mfma_f32_16x16x32_bf16 v[56:59], v[84:87], v[124:127], v[56:59]\n"
    "v_mfma_f32_16x16x32_bf16 v[52:55], v[72:75], v[120:123], v[52:55]\n"
    "v_mfma_f32_16x16x32_bf16 v[52:55], v[76:79], v[124:127], v[52:55]\n"
    "v_mfma_f32_16x16x32_bf16 v[48:51], v[64:67], v[120:123], v[48:51]\n"
    "v_mfma_f32_16x16x32_bf16 v[48:51], v[68:71], v[124:127], v[48:51]\n"
    "s_waitcnt lgkmcnt(0)\n"
    "s_barrier\n"
    "v_mfma_f32_16x16x32_bf16 v[0:3], v[128:131], v[160:163], v[0:3]\n"
    "ds_read_b128 v[96:99], v222 offset:32768\n"
    "v_mfma_f32_16x16x32_bf16 v[0:3], v[132:135], v[164:167], v[0:3]\n"
    "ds_read_b128 v[100:103], v223 offset:32768\n"
    "v_mfma_f32_16x16x32_bf16 v[4:7], v[136:139], v[160:163], v[4:7]\n"
    "ds_read_b128 v[64:67], v224 offset:32768\n"
    "v_mfma_f32_16x16x32_bf16 v[4:7], v[140:143], v[164:167], v[4:7]\n"
    "ds_read_b128 v[68:71], v224 offset:33792\n"
    "v_mfma_f32_16x16x32_bf16 v[8:11], v[144:147], v[160:163], v[8:11]\n"
    "ds_read_b128 v[72:75], v224 offset:34816\n"
    "v_mfma_f32_16x16x32_bf16 v[8:11], v[148:151], v[164:167], v[8:11]\n"
    "ds_read_b128 v[76:79], v224 offset:35840\n"
    "v_mfma_f32_16x16x32_bf16 v[12:15], v[152:155], v[160:163], v[12:15]\n"
    "ds_read_b128 v[80:83], v224 offset:36864\n"
    "v_mfma_f32_16x16x32_bf16 v[12:15], v[156:159], v[164:167], v[12:15]\n"
    "ds_read_b128 v[84:87], v224 offset:37888\n"
    "v_mfma_f32_16x16x32_bf16 v[28:31], v[152:155], v[168:171], v[28:31]\n"
    "ds_read_b128 v[88:91], v224 offset:38912\n"
    "v_mfma_f32_16x16x32_bf16 v[28:31], v[156:159], v[172:175], v[28:31]\n"
    "ds_read_b128 v[92:95], v224 offset:39936\n"
    "v_mfma_f32_16x16x32_bf16 v[24:27], v[144:147], v[168:171], v[24:27]\n"
    "ds_read_b128 v[104:107], v222 offset:34816\n"
    "v_mfma_f32_16x16x32_bf16 v[24:27], v[148:151], v[172:175], v[24:27]\n"
    "ds_read_b128 v[108:111], v223 offset:34816\n"
    "v_mfma_f32_16x16x32_bf16 v[20:23], v[136:139], v[168:171], v[20:23]\n"
    "ds_read_b128 v[112:115], v222 offset:36864\n"
    "v_mfma_f32_16x16x32_bf16 v[20:23], v[140:143], v[172:175], v[20:23]\n"
    "ds_read_b128 v[116:119], v223 offset:36864\n"
    "v_mfma_f32_16x16x32_bf16 v[16:19], v[128:131], v[168:171], v[16:19]\n"
    "ds_read_b128 v[120:123], v222 offset:38912\n"
    "v_mfma_f32_16x16x32_bf16 v[16:19], v[132:135], v[172:175], v[16:19]\n"
    "ds_read_b128 v[124:127], v223 offset:38912\n"
    "v_mfma_f32_16x16x32_bf16 v[32:35], v[128:131], v[176:179], v[32:35]\n"
    "v_mfma_f32_16x16x32_bf16 v[32:35], v[132:135], v[180:183], v[32:35]\n"
    "v_mfma_f32_16x16x32_bf16 v[36:39], v[136:139], v[176:179], v[36:39]\n"
    "v_mfma_f32_16x16x32_bf16 v[36:39], v[140:143], v[180:183], v[36:39]\n"
    "v_mfma_f32_16x16x32_bf16 v[40:43], v[144:147], v[176:179], v[40:43]\n"
    "v_mfma_f32_16x16x32_bf16 v[40:43], v[148:151], v[180:183], v[40:43]\n"
    "v_mfma_f32_16x16x32_bf16 v[44:47], v[152:155], v[176:179], v[44:47]\n"
    "v_mfma_f32_16x16x32_bf16 v[44:47], v[156:159], v[180:183], v[44:47]\n"
    "v_mfma_f32_16x16x32_bf16 v[60:63], v[152:155], v[184:187], v[60:63]\n"
    "v_mfma_f32_16x16x32_bf16 v[60:63], v[156:159], v[188:191], v[60:63]\n"
    "v_mfma_f32_16x16x32_bf16 v[56:59], v[144:147], v[184:187], v[56:59]\n"
    "v_mfma_f32_16x16x32_bf16 v[56:59], v[148:151], v[188:191], v[56:59]\n"
    "v_mfma_f32_16x16x32_bf16 v[52:55], v[136:139], v[184:187], v[52:55]\n"
    "v_mfma_f32_16x16x32_bf16 v[52:55], v[140:143], v[188:191], v[52:55]\n"
    "v_mfma_f32_16x16x32_bf16 v[48:51], v[128:131], v[184:187], v[48:51]\n"
    "v_mfma_f32_16x16x32_bf16 v[48:51], v[132:135], v[188:191], v[48:51]\n"
    "s_waitcnt lgkmcnt(0)\n"
    "s_barrier\n"
    "v_mfma_f32_16x16x32_bf16 v[0:3], v[64:67], v[96:99], v[0:3]\n"
    "ds_read_b128 v[160:163], v220 offset:32768\n"
    "v_mfma_f32_16x16x32_bf16 v[0:3], v[68:71], v[100:103], v[0:3]\n"
    "ds_read_b128 v[164:167], v221 offset:32768\n"
    "v_mfma_f32_16x16x32_bf16 v[4:7], v[72:75], v[96:99], v[4:7]\n"
    "ds_read_b128 v[128:131], v224 offset:49152\n"
    "v_mfma_f32_16x16x32_bf16 v[4:7], v[76:79], v[100:103], v[4:7]\n"
    "ds_read_b128 v[132:135], v224 offset:50176\n"
    "v_mfma_f32_16x16x32_bf16 v[8:11], v[80:83], v[96:99], v[8:11]\n"
    "ds_read_b128 v[136:139], v224 offset:51200\n"
    "v_mfma_f32_16x16x32_bf16 v[8:11], v[84:87], v[100:103], v[8:11]\n"
    "ds_read_b128 v[140:143], v224 offset:52224\n"
    "v_mfma_f32_16x16x32_bf16 v[12:15], v[88:91], v[96:99], v[12:15]\n"
    "ds_read_b128 v[144:147], v224 offset:53248\n"
    "v_mfma_f32_16x16x32_bf16 v[12:15], v[92:95], v[100:103], v[12:15]\n"
    "ds_read_b128 v[148:151], v224 offset:54272\n"
    "v_mfma_f32_16x16x32_bf16 v[28:31], v[88:91], v[104:107], v[28:31]\n"
    "ds_read_b128 v[152:155], v224 offset:55296\n"
    "v_mfma_f32_16x16x32_bf16 v[28:31], v[92:95], v[108:111], v[28:31]\n"
    "ds_read_b128 v[156:159], v224 offset:56320\n"
    ".LPAD1_%=:\n"
    "s_cbranch_execz .LRET_%=\n"
    "s_nop 0\n"
    "v_mfma_f32_16x16x32_bf16 v[24:27], v[80:83], v[104:107], v[24:27]\n"
    "ds_read_b128 v[168:171], v220 offset:34816\n"
    "v_mfma_f32_16x16x32_bf16 v[24:27], v[84:87], v[108:111], v[24:27]\n"
    "ds_read_b128 v[172:175], v221 offset:34816\n"
    "v_mfma_f32_16x16x32_bf16 v[20:23], v[72:75], v[104:107], v[20:23]\n"
    "ds_read_b128 v[176:179], v220 offset:36864\n"
    "v_mfma_f32_16x16x32_bf16 v[20:23], v[76:79], v[108:111], v[20:23]\n"
    "ds_read_b128 v[180:183], v221 offset:36864\n"
    "v_mfma_f32_16x16x32_bf16 v[16:19], v[64:67], v[104:107], v[16:19]\n"
    "ds_read_b128 v[184:187], v220 offset:38912\n"
    "v_mfma_f32_16x16x32_bf16 v[16:19], v[68:71], v[108:111], v[16:19]\n"
    "ds_read_b128 v[188:191], v221 offset:38912\n"
    "v_mfma_f32_16x16x32_bf16 v[32:35], v[64:67], v[112:115], v[32:35]\n"
    "v_mfma_f32_16x16x32_bf16 v[32:35], v[68:71], v[116:119], v[32:35]\n"
    "v_mfma_f32_16x16x32_bf16 v[36:39], v[72:75], v[112:115], v[36:39]\n"
    "v_mfma_f32_16x16x32_bf16 v[36:39], v[76:79], v[116:119], v[36:39]\n"
    "v_mfma_f32_16x16x32_bf16 v[40:43], v[80:83], v[112:115], v[40:43]\n"
    "v_mfma_f32_16x16x32_bf16 v[40:43], v[84:87], v[116:119], v[40:43]\n"
    "v_mfma_f32_16x16x32_bf16 v[44:47], v[88:91], v[112:115], v[44:47]\n"
    "v_mfma_f32_16x16x32_bf16 v[44:47], v[92:95], v[116:119], v[44:47]\n"
    "v_mfma_f32_16x16x32_bf16 v[60:63], v[88:91], v[120:123], v[60:63]\n"
    "v_mfma_f32_16x16x32_bf16 v[60:63], v[92:95], v[124:127], v[60:63]\n"
    "v_mfma_f32_16x16x32_bf16 v[56:59], v[80:83], v[120:123], v[56:59]\n"
    "v_mfma_f32_16x16x32_bf16 v[56:59], v[84:87], v[124:127], v[56:59]\n"
    "v_mfma_f32_16x16x32_bf16 v[52:55], v[72:75], v[120:123], v[52:55]\n"
    "v_mfma_f32_16x16x32_bf16 v[52:55], v[76:79], v[124:127], v[52:55]\n"
    "v_mfma_f32_16x16x32_bf16 v[48:51], v[64:67], v[120:123], v[48:51]\n"
    "v_mfma_f32_16x16x32_bf16 v[48:51], v[68:71], v[124:127], v[48:51]\n"
    "s_waitcnt lgkmcnt(0)\n"
    "s_barrier\n"
    "v_mfma_f32_16x16x32_bf16 v[0:3], v[128:131], v[160:163], v[0:3]\n"
    "ds_read_b128 v[96:99], v222 offset:0\n"
    "v_mfma_f32_16x16x32_bf16 v[0:3], v[132:135], v[164:167], v[0:3]\n"
    "ds_read_b128 v[100:103], v223 offset:0\n"
    "v_mfma_f32_16x16x32_bf16 v[4:7], v[136:139], v[160:163], v[4:7]\n"
    "ds_read_b128 v[64:67], v224 offset:0\n"
    "v_mfma_f32_16x16x32_bf16 v[4:7], v[140:143], v[164:167], v[4:7]\n"
    "ds_read_b128 v[68:71], v224 offset:1024\n"
    "v_mfma_f32_16x16x32_bf16 v[8:11], v[144:147], v[160:163], v[8:11]\n"
    "ds_read_b128 v[72:75], v224 offset:2048\n"
    "v_mfma_f32_16x16x32_bf16 v[8:11], v[148:151], v[164:167], v[8:11]\n"
    "ds_read_b128 v[76:79], v224 offset:3072\n"
    "v_mfma_f32_16x16x32_bf16 v[12:15], v[152:155], v[160:163], v[12:15]\n"
    "ds_read_b128 v[80:83], v224 offset:4096\n"
    "v_mfma_f32_16x16x32_bf16 v[12:15], v[156:159], v[164:167], v[12:15]\n"
    "ds_read_b128 v[84:87], v224 offset:5120\n"
    "v_mfma_f32_16x16x32_bf16 v[28:31], v[152:155], v[168:171], v[28:31]\n"
    "ds_read_b128 v[88:91], v224 offset:6144\n"
    "v_mfma_f32_16x16x32_bf16 v[28:31], v[156:159], v[172:175], v[28:31]\n"
    "ds_read_b128 v[92:95], v224 offset:7168\n"
    "v_mfma_f32_16x16x32_bf16 v[24:27], v[144:147], v[168:171], v[24:27]\n"
    "ds_read_b128 v[104:107], v222 offset:2048\n"
    "v_mfma_f32_16x16x32_bf16 v[24:27], v[148:151], v[172:175], v[24:27]\n"
    "ds_read_b128 v[108:111], v223 offset:2048\n"
    "v_mfma_f32_16x16x32_bf16 v[20:23], v[136:139], v[168:171], v[20:23]\n"
    "ds_read_b128 v[112:115], v222 offset:4096\n"
    "v_mfma_f32_16x16x32_bf16 v[20:23], v[140:143], v[172:175], v[20:23]\n"
    "ds_read_b128 v[116:119], v223 offset:4096\n"
    "v_mfma_f32_16x16x32_bf16 v[16:19], v[128:131], v[168:171], v[16:19]\n"
    "ds_read_b128 v[120:123], v222 offset:6144\n"
    "v_mfma_f32_16x16x32_bf16 v[16:19], v[132:135], v[172:175], v[16:19]\n"
    "ds_read_b128 v[124:127], v223 offset:6144\n"
    "v_mfma_f32_16x16x32_bf16 v[32:35], v[128:131], v[176:179], v[32:35]\n"
    "v_mfma_f32_16x16x32_bf16 v[32:35], v[132:135], v[180:183], v[32:35]\n"
    "v_mfma_f32_16x16x32_bf16 v[36:39], v[136:139], v[176:179], v[36:39]\n"
    "v_mfma_f32_16x16x32_bf16 v[36:39], v[140:143], v[180:183], v[36:39]\n"
    "v_mfma_f32_16x16x32_bf16 v[40:43], v[144:147], v[176:179], v[40:43]\n"
    "v_mfma_f32_16x16x32_bf16 v[40:43], v[148:151], v[180:183], v[40:43]\n"
    "v_mfma_f32_16x16x32_bf16 v[44:47], v[152:155], v[176:179], v[44:47]\n"
    "v_mfma_f32_16x16x32_bf16 v[44:47], v[156:159], v[180:183], v[44:47]\n"
    "v_mfma_f32_16x16x32_bf16 v[60:63], v[152:155], v[184:187], v[60:63]\n"
    "v_mfma_f32_16x16x32_bf16 v[60:63], v[156:159], v[188:191], v[60:63]\n"
    "v_mfma_f32_16x16x32_bf16 v[56:59], v[144:147], v[184:187], v[56:59]\n"
    "v_mfma_f32_16x16x32_bf16 v[56:59], v[148:151], v[188:191], v[56:59]\n"
    "v_mfma_f32_16x16x32_bf16 v[52:55], v[136:139], v[184:187], v[52:55]\n"
    "v_mfma_f32_16x16x32_bf16 v[52:55], v[140:143], v[188:191], v[52:55]\n"
    "v_mfma_f32_16x16x32_bf16 v[48:51], v[128:131], v[184:187], v[48:51]\n"
    "v_mfma_f32_16x16x32_bf16 v[48:51], v[132:135], v[188:191], v[48:51]\n"
    "s_waitcnt lgkmcnt(0)\n"
    "s_barrier\n"
    "v_mfma_f32_16x16x32_bf16 v[0:3], v[64:67], v[96:99], v[0:3]\n"
    "ds_read_b128 v[160:163], v222 offset:16384\n"
    "v_mfma_f32_16x16x32_bf16 v[0:3], v[68:71], v[100:103], v[0:3]\n"
    "ds_read_b128 v[164:167], v223 offset:16384\n"
    "v_mfma_f32_16x16x32_bf16 v[4:7], v[72:75], v[96:99], v[4:7]\n"
    "ds_read_b128 v[128:131], v224 offset:16384\n"
    "v_mfma_f32_16x16x32_bf16 v[4:7], v[76:79], v[100:103], v[4:7]\n"
    "ds_read_b128 v[132:135], v224 offset:17408\n"
    "v_mfma_f32_16x16x32_bf16 v[8:11], v[80:83], v[96:99], v[8:11]\n"
    "ds_read_b128 v[136:139], v224 offset:18432\n"
    "v_mfma_f32_16x16x32_bf16 v[8:11], v[84:87], v[100:103], v[8:11]\n"
    "ds_read_b128 v[140:143], v224 offset:19456\n"
    "v_mfma_f32_16x16x32_bf16 v[12:15], v[88:91], v[96:99], v[12:15]\n"
    "ds_read_b128 v[144:147], v224 offset:20480\n"
    "v_mfma_f32_16x16x32_bf16 v[12:15], v[92:95], v[100:103], v[12:15]\n"
    "ds_read_b128 v[148:151], v224 offset:21504\n"
    "v_mfma_f32_16x16x32_bf16 v[28:31], v[88:91], v[104:107], v[28:31]\n"
    "ds_read_b128 v[152:155], v224 offset:22528\n"
    "v_mfma_f32_16x16x32_bf16 v[28:31], v[92:95], v[108:111], v[28:31]\n"
    "ds_read_b128 v[156:159], v224 offset:23552\n"
    "v_mfma_f32_16x16x32_bf16 v[24:27], v[80:83], v[104:107], v[24:27]\n"
    "ds_read_b128 v[168:171], v222 offset:18432\n"
    "v_mfma_f32_16x16x32_bf16 v[24:27], v[84:87], v[108:111], v[24:27]\n"
    "ds_read_b128 v[172:175], v223 offset:18432\n"
    "v_mfma_f32_16x16x32_bf16 v[20:23], v[72:75], v[104:107], v[20:23]\n"
    "ds_read_b128 v[176:179], v222 offset:20480\n"
    "v_mfma_f32_16x16x32_bf16 v[20:23], v[76:79], v[108:111], v[20:23]\n"
    "ds_read_b128 v[180:183], v223 offset:20480\n"
    "v_mfma_f32_16x16x32_bf16 v[16:19], v[64:67], v[104:107], v[16:19]\n"
    "ds_read_b128 v[184:187], v222 offset:22528\n"
    "v_mfma_f32_16x16x32_bf16 v[16:19], v[68:71], v[108:111], v[16:19]\n"
    "ds_read_b128 v[188:191], v223 offset:22528\n"
    "v_mfma_f32_16x16x32_bf16 v[32:35], v[64:67], v[112:115], v[32:35]\n"
    "v_mfma_f32_16x16x32_bf16 v[32:35], v[68:71], v[116:119], v[32:35]\n"
    "v_mfma_f32_16x16x32_bf16 v[36:39], v[72:75], v[112:115], v[36:39]\n"
    "v_mfma_f32_16x16x32_bf16 v[36:39], v[76:79], v[116:119], v[36:39]\n"
    "v_mfma_f32_16x16x32_bf16 v[40:43], v[80:83], v[112:115], v[40:43]\n"
    "v_mfma_f32_16x16x32_bf16 v[40:43], v[84:87], v[116:119], v[40:43]\n"
    "v_mfma_f32_16x16x32_bf16 v[44:47], v[88:91], v[112:115], v[44:47]\n"
    "v_mfma_f32_16x16x32_bf16 v[44:47], v[92:95], v[116:119], v[44:47]\n"
    "v_mfma_f32_16x16x32_bf16 v[60:63], v[88:91], v[120:123], v[60:63]\n"
    "v_mfma_f32_16x16x32_bf16 v[60:63], v[92:95], v[124:127], v[60:63]\n"
    "v_mfma_f32_16x16x32_bf16 v[56:59], v[80:83], v[120:123], v[56:59]\n"
    "v_mfma_f32_16x16x32_bf16 v[56:59], v[84:87], v[124:127], v[56:59]\n"
    "v_mfma_f32_16x16x32_bf16 v[52:55], v[72:75], v[120:123], v[52:55]\n"
    "v_mfma_f32_16x16x32_bf16 v[52:55], v[76:79], v[124:127], v[52:55]\n"
    "v_mfma_f32_16x16x32_bf16 v[48:51], v[64:67], v[120:123], v[48:51]\n"
    "v_mfma_f32_16x16x32_bf16 v[48:51], v[68:71], v[124:127], v[48:51]\n"
    "s_waitcnt lgkmcnt(0)\n"
    "s_barrier\n"
    "v_mfma_f32_16x16x32_bf16 v[0:3], v[128:131], v[160:163], v[0:3]\n"
    "ds_read_b128 v[96:99], v222 offset:32768\n"
    "v_mfma_f32_16x16x32_bf16 v[0:3], v[132:135], v[164:167], v[0:3]\n"
    "ds_read_b128 v[100:103], v223 offset:32768\n"
    "v_mfma_f32_16x16x32_bf16 v[4:7], v[136:139], v[160:163], v[4:7]\n"
    "ds_read_b128 v[64:67], v224 offset:32768\n"
    "v_mfma_f32_16x16x32_bf16 v[4:7], v[140:143], v[164:167], v[4:7]\n"
    "ds_read_b128 v[68:71], v224 offset:33792\n"
    "v_mfma_f32_16x16x32_bf16 v[8:11], v[144:147], v[160:163], v[8:11]\n"
    "ds_read_b128 v[72:75], v224 offset:34816\n"
    "v_mfma_f32_16x16x32_bf16 v[8:11], v[148:151], v[164:167], v[8:11]\n"
    "ds_read_b128 v[76:79], v224 offset:35840\n"
    "v_mfma_f32_16x16x32_bf16 v[12:15], v[152:155], v[160:163], v[12:15]\n"
    "ds_read_b128 v[80:83], v224 offset:36864\n"
    "v_mfma_f32_16x16x32_bf16 v[12:15], v[156:159], v[164:167], v[12:15]\n"
    "ds_read_b128 v[84:87], v224 offset:37888\n"
    "v_mfma_f32_16x16x32_bf16 v[28:31], v[152:155], v[168:171], v[28:31]\n"
    "ds_read_b128 v[88:91], v224 offset:38912\n"
    "v_mfma_f32_16x16x32_bf16 v[28:31], v[156:159], v[172:175], v[28:31]\n"
    "ds_read_b128 v[92:95], v224 offset:39936\n"
    "v_mfma_f32_16x16x32_bf16 v[24:27], v[144:147], v[168:171], v[24:27]\n"
    "ds_read_b128 v[104:107], v222 offset:34816\n"
    "v_mfma_f32_16x16x32_bf16 v[24:27], v[148:151], v[172:175], v[24:27]\n"
    "ds_read_b128 v[108:111], v223 offset:34816\n"
    "v_mfma_f32_16x16x32_bf16 v[20:23], v[136:139], v[168:171], v[20:23]\n"
    "ds_read_b128 v[112:115], v222 offset:36864\n"
    "v_mfma_f32_16x16x32_bf16 v[20:23], v[140:143], v[172:175], v[20:23]\n"
    "ds_read_b128 v[116:119], v223 offset:36864\n"
    "v_mfma_f32_16x16x32_bf16 v[16:19], v[128:131], v[168:171], v[16:19]\n"
    "ds_read_b128 v[120:123], v222 offset:38912\n"
    "v_mfma_f32_16x16x32_bf16 v[16:19], v[132:135], v[172:175], v[16:19]\n"
    "ds_read_b128 v[124:127], v223 offset:38912\n"
    "v_mfma_f32_16x16x32_bf16 v[32:35], v[128:131], v[176:179], v[32:35]\n"
    "v_mfma_f32_16x16x32_bf16 v[32:35], v[132:135], v[180:183], v[32:35]\n"
    "v_mfma_f32_16x16x32_bf16 v[36:39], v[136:139], v[176:179], v[36:39]\n"
    "v_mfma_f32_16x16x32_bf16 v[36:39], v[140:143], v[180:183], v[36:39]\n"
    "v_mfma_f32_16x16x32_bf16 v[40:43], v[144:147], v[176:179], v[40:43]\n"
    "v_mfma_f32_16x16x32_bf16 v[40:43], v[148:151], v[180:183], v[40:43]\n"
    "v_mfma_f32_16x16x32_bf16 v[44:47], v[152:155], v[176:179], v[44:47]\n"
    "v_mfma_f32_16x16x32_bf16 v[44:47], v[156:159], v[180:183], v[44:47]\n"
    "v_mfma_f32_16x16x32_bf16 v[60:63], v[152:155], v[184:187], v[60:63]\n"
    "v_mfma_f32_16x16x32_bf16 v[60:63], v[156:159], v[188:191], v[60:63]\n"
    "v_mfma_f32_16x16x32_bf16 v[56:59], v[144:147], v[184:187], v[56:59]\n"
    "v_mfma_f32_16x16x32_bf16 v[56:59], v[148:151], v[188:191], v[56:59]\n"
    "v_mfma_f32_16x16x32_bf16 v[52:55], v[136:139], v[184:187], v[52:55]\n"
    "v_mfma_f32_16x16x32_bf16 v[52:55], v[140:143], v[188:191], v[52:55]\n"
    "v_mfma_f32_16x16x32_bf16 v[48:51], v[128:131], v[184:187], v[48:51]\n"
    "v_mfma_f32_16x16x32_bf16 v[48:51], v[132:135], v[188:191], v[48:51]\n"
    "s_waitcnt lgkmcnt(0)\n"
    "s_barrier\n"
    "v_mfma_f32_16x16x32_bf16 v[0:3], v[64:67], v[96:99], v[0:3]\n"
    "ds_read_b128 v[160:163], v220 offset:32768\n"
    "v_mfma_f32_16x16x32_bf16 v[0:3], v[68:71], v[100:103], v[0:3]\n"
    "ds_read_b128 v[164:167], v221 offset:32768\n"
    "v_mfma_f32_16x16x32_bf16 v[4:7], v[72:75], v[96:99], v[4:7]\n"
    "ds_read_b128 v[128:131], v224 offset:49152\n"
    "v_mfma_f32_16x16x32_bf16 v[4:7], v[76:79], v[100:103], v[4:7]\n"
    "ds_read_b128 v[132:135], v224 offset:50176\n"
    "v_mfma_f32_16x16x32_bf16 v[8:11], v[80:83], v[96:99], v[8:11]\n"
    "ds_read_b128 v[136:139], v224 offset:51200\n"
    "v_mfma_f32_16x16x32_bf16 v[8:11], v[84:87], v[100:103], v[8:11]\n"
    "ds_read_b128 v[140:143], v224 offset:52224\n"
    "v_mfma_f32_16x16x32_bf16 v[12:15], v[88:91], v[96:99], v[12:15]\n"
    "ds_read_b128 v[144:147], v224 offset:53248\n"
    "v_mfma_f32_16x16x32_bf16 v[12:15], v[92:95], v[100:103], v[12:15]\n"
    "ds_read_b128 v[148:151], v224 offset:54272\n"
    "v_mfma_f32_16x16x32_bf16 v[28:31], v[88:91], v[104:107], v[28:31]\n"
    "ds_read_b128 v[152:155], v224 offset:55296\n"
    "v_mfma_f32_16x16x32_bf16 v[28:31], v[92:95], v[108:111], v[28:31]\n"
    "ds_read_b128 v[156:159], v224 offset:56320\n"
    "v_mfma_f32_16x16x32_bf16 v[24:27], v[80:83], v[104:107], v[24:27]\n"
    "ds_read_b128 v[168:171], v220 offset:34816\n"
    "v_mfma_f32_16x16x32_bf16 v[24:27], v[84:87], v[108:111], v[24:27]\n"
    "ds_read_b128 v[172:175], v221 offset:34816\n"
    "v_mfma_f32_16x16x32_bf16 v[20:23], v[72:75], v[104:107], v[20:23]\n"
    "ds_read_b128 v[176:179], v220 offset:36864\n"
    "v_mfma_f32_16x16x32_bf16 v[20:23], v[76:79], v[108:111], v[20:23]\n"
    "ds_read_b128 v[180:183], v221 offset:36864\n"
    "v_mfma_f32_16x16x32_bf16 v[16:19], v[64:67], v[104:107], v[16:19]\n"
    "ds_read_b128 v[184:187], v220 offset:38912\n"
    "v_mfma_f32_16x16x32_bf16 v[16:19], v[68:71], v[108:111], v[16:19]\n"
    "ds_read_b128 v[188:191], v221 offset:38912\n"
    "v_mfma_f32_16x16x32_bf16 v[32:35], v[64:67], v[112:115], v[32:35]\n"
    "v_mfma_f32_16x16x32_bf16 v[32:35], v[68:71], v[116:119], v[32:35]\n"
    "v_mfma_f32_16x16x32_bf16 v[36:39], v[72:75], v[112:115], v[36:39]\n"
    "v_mfma_f32_16x16x32_bf16 v[36:39], v[76:79], v[116:119], v[36:39]\n"
    "v_mfma_f32_16x16x32_bf16 v[40:43], v[80:83], v[112:115], v[40:43]\n"
    "v_mfma_f32_16x16x32_bf16 v[40:43], v[84:87], v[116:119], v[40:43]\n"
    "v_mfma_f32_16x16x32_bf16 v[44:47], v[88:91], v[112:115], v[44:47]\n"
    "v_mfma_f32_16x16x32_bf16 v[44:47], v[92:95], v[116:119], v[44:47]\n"
    "v_mfma_f32_16x16x32_bf16 v[60:63], v[88:91], v[120:123], v[60:63]\n"
    "v_mfma_f32_16x16x32_bf16 v[60:63], v[92:95], v[124:127], v[60:63]\n"
    "v_mfma_f32_16x16x32_bf16 v[56:59], v[80:83], v[120:123], v[56:59]\n"
    "v_mfma_f32_16x16x32_bf16 v[56:59], v[84:87], v[124:127], v[56:59]\n"
    "v_mfma_f32_16x16x32_bf16 v[52:55], v[72:75], v[120:123], v[52:55]\n"
    "v_mfma_f32_16x16x32_bf16 v[52:55], v[76:79], v[124:127], v[52:55]\n"
    "v_mfma_f32_16x16x32_bf16 v[48:51], v[64:67], v[120:123], v[48:51]\n"
    "v_mfma_f32_16x16x32_bf16 v[48:51], v[68:71], v[124:127], v[48:51]\n"
    "s_waitcnt lgkmcnt(0)\n"
    "s_barrier\n"
    "v_mfma_f32_16x16x32_bf16 v[0:3], v[128:131], v[160:163], v[0:3]\n"
    "ds_read_b128 v[96:99], v222 offset:0\n"
    "v_mfma_f32_16x16x32_bf16 v[0:3], v[132:135], v[164:167], v[0:3]\n"
    "ds_read_b128 v[100:103], v223 offset:0\n"
    "v_mfma_f32_16x16x32_bf16 v[4:7], v[136:139], v[160:163], v[4:7]\n"
    "ds_read_b128 v[64:67], v224 offset:0\n"
    "v_mfma_f32_16x16x32_bf16 v[4:7], v[140:143], v[164:167], v[4:7]\n"
    "ds_read_b128 v[68:71], v224 offset:1024\n"
    "v_mfma_f32_16x16x32_bf16 v[8:11], v[144:147], v[160:163], v[8:11]\n"
    "ds_read_b128 v[72:75], v224 offset:2048\n"
    "v_mfma_f32_16x16x32_bf16 v[8:11], v[148:151], v[164:167], v[8:11]\n"
    "ds_read_b128 v[76:79], v224 offset:3072\n"
    "v_mfma_f32_16x16x32_bf16 v[12:15], v[152:155], v[160:163], v[12:15]\n"
    "ds_read_b128 v[80:83], v224 offset:4096\n"
    "v_mfma_f32_16x16x32_bf16 v[12:15], v[156:159], v[164:167], v[12:15]\n"
    "ds_read_b128 v[84:87], v224 offset:5120\n"
    "v_mfma_f32_16x16x32_bf16 v[28:31], v[152:155], v[168:171], v[28:31]\n"
    "ds_read_b128 v[88:91], v224 offset:6144\n"
    "v_mfma_f32_16x16x32_bf16 v[28:31], v[156:159], v[172:175], v[28:31]\n"
    "ds_read_b128 v[92:95], v224 offset:7168\n"
    "v_mfma_f32_16x16x32_bf16 v[24:27], v[144:147], v[168:171], v[24:27]\n"
    "ds_read_b128 v[104:107], v222 offset:2048\n"
    "v_mfma_f32_16x16x32_bf16 v[24:27], v[148:151], v[172:175], v[24:27]\n"
    "ds_read_b128 v[108:111], v223 offset:2048\n"
    "v_mfma_f32_16x16x32_bf16 v[20:23], v[136:139], v[168:171], v[20:23]\n"
    "ds_read_b128 v[112:115], v222 offset:4096\n"
    "v_mfma_f32_16x16x32_bf16 v[20:23], v[140:143], v[172:175], v[20:23]\n"
    "ds_read_b128 v[116:119], v223 offset:4096\n"
    "v_mfma_f32_16x16x32_bf16 v[16:19], v[128:131], v[168:171], v[16:19]\n"
    "ds_read_b128 v[120:123], v222 offset:6144\n"
    "v_mfma_f32_16x16x32_bf16 v[16:19], v[132:135], v[172:175], v[16:19]\n"
    "ds_read_b128 v[124:127], v223 offset:6144\n"
    "v_mfma_f32_16x16x32_bf16 v[32:35], v[128:131], v[176:179], v[32:35]\n"
    "v_mfma_f32_16x16x32_bf16 v[32:35], v[132:135], v[180:183], v[32:35]\n"
    "v_mfma_f32_16x16x32_bf16 v[36:39], v[136:139], v[176:179], v[36:39]\n"
    "v_mfma_f32_16x16x32_bf16 v[36:39], v[140:143], v[180:183], v[36:39]\n"
    "v_mfma_f32_16x16x32_bf16 v[40:43], v[144:147], v[176:179], v[40:43]\n"
    "v_mfma_f32_16x16x32_bf16 v[40:43], v[148:151], v[180:183], v[40:43]\n"
    "v_mfma_f32_16x16x32_bf16 v[44:47], v[152:155], v[176:179], v[44:47]\n"
    "v_mfma_f32_16x16x32_bf16 v[44:47], v[156:159], v[180:183], v[44:47]\n"
    "v_mfma_f32_16x16x32_bf16 v[60:63], v[152:155], v[184:187], v[60:63]\n"
    "v_mfma_f32_16x16x32_bf16 v[60:63], v[156:159], v[188:191], v[60:63]\n"
    "v_mfma_f32_16x16x32_bf16 v[56:59], v[144:147], v[184:187], v[56:59]\n"
    "v_mfma_f32_16x16x32_bf16 v[56:59], v[148:151], v[188:191], v[56:59]\n"
    "v_mfma_f32_16x16x32_bf16 v[52:55], v[136:139], v[184:187], v[52:55]\n"
    "v_mfma_f32_16x16x32_bf16 v[52:55], v[140:143], v[188:191], v[52:55]\n"
    "v_mfma_f32_16x16x32_bf16 v[48:51], v[128:131], v[184:187], v[48:51]\n"
    "v_mfma_f32_16x16x32_bf16 v[48:51], v[132:135], v[188:191], v[48:51]\n"
    "s_waitcnt lgkmcnt(0)\n"
    "s_barrier\n"
    "v_mfma_f32_16x16x32_bf16 v[0:3], v[64:67], v[96:99], v[0:3]\n"
    "ds_read_b128 v[160:163], v222 offset:16384\n"
    "v_mfma_f32_16x16x32_bf16 v[0:3], v[68:71], v[100:103], v[0:3]\n"
    "ds_read_b128 v[164:167], v223 offset:16384\n"
    "v_mfma_f32_16x16x32_bf16 v[4:7], v[72:75], v[96:99], v[4:7]\n"
    "ds_read_b128 v[128:131], v224 offset:16384\n"
    "v_mfma_f32_16x16x32_bf16 v[4:7], v[76:79], v[100:103], v[4:7]\n"
    "ds_read_b128 v[132:135], v224 offset:17408\n"
    "v_mfma_f32_16x16x32_bf16 v[8:11], v[80:83], v[96:99], v[8:11]\n"
    "ds_read_b128 v[136:139], v224 offset:18432\n"
    "v_mfma_f32_16x16x32_bf16 v[8:11], v[84:87], v[100:103], v[8:11]\n"
    "ds_read_b128 v[140:143], v224 offset:19456\n"
    "v_mfma_f32_16x16x32_bf16 v[12:15], v[88:91], v[96:99], v[12:15]\n"
    "ds_read_b128 v[144:147], v224 offset:20480\n"
    "v_mfma_f32_16x16x32_bf16 v[12:15], v[92:95], v[100:103], v[12:15]\n"
    "ds_read_b128 v[148:151], v224 offset:21504\n"
    "v_mfma_f32_16x16x32_bf16 v[28:31], v[88:91], v[104:107], v[28:31]\n"
    "ds_read_b128 v[152:155], v224 offset:22528\n"
    "v_mfma_f32_16x16x32_bf16 v[28:31], v[92:95], v[108:111], v[28:31]\n"
    "ds_read_b128 v[156:159], v224 offset:23552\n"
    "v_mfma_f32_16x16x32_bf16 v[24:27], v[80:83], v[104:107], v[24:27]\n"
    "ds_read_b128 v[168:171], v222 offset:18432\n"
    "v_mfma_f32_16x16x32_bf16 v[24:27], v[84:87], v[108:111], v[24:27]\n"
    "ds_read_b128 v[172:175], v223 offset:18432\n"
    "v_mfma_f32_16x16x32_bf16 v[20:23], v[72:75], v[104:107], v[20:23]\n"
    "ds_read_b128 v[176:179], v222 offset:20480\n"
    "v_mfma_f32_16x16x32_bf16 v[20:23], v[76:79], v[108:111], v[20:23]\n"
    "ds_read_b128 v[180:183], v223 offset:20480\n"
    "v_mfma_f32_16x16x32_bf16 v[16:19], v[64:67], v[104:107], v[16:19]\n"
    "ds_read_b128 v[184:187], v222 offset:22528\n"
    "v_mfma_f32_16x16x32_bf16 v[16:19], v[68:71], v[108:111], v[16:19]\n"
    "ds_read_b128 v[188:191], v223 offset:22528\n"
    "v_mfma_f32_16x16x32_bf16 v[32:35], v[64:67], v[112:115], v[32:35]\n"
    "v_mfma_f32_16x16x32_bf16 v[32:35], v[68:71], v[116:119], v[32:35]\n"
    "v_mfma_f32_16x16x32_bf16 v[36:39], v[72:75], v[112:115], v[36:39]\n"
    "v_mfma_f32_16x16x32_bf16 v[36:39], v[76:79], v[116:119], v[36:39]\n"
    "v_mfma_f32_16x16x32_bf16 v[40:43], v[80:83], v[112:115], v[40:43]\n"
    "v_mfma_f32_16x16x32_bf16 v[40:43], v[84:87], v[116:119], v[40:43]\n"
    "v_mfma_f32_16x16x32_bf16 v[44:47], v[88:91], v[112:115], v[44:47]\n"
    "v_mfma_f32_16x16x32_bf16 v[44:47], v[92:95], v[116:119], v[44:47]\n"
    "v_mfma_f32_16x16x32_bf16 v[60:63], v[88:91], v[120:123], v[60:63]\n"
    "v_mfma_f32_16x16x32_bf16 v[60:63], v[92:95], v[124:127], v[60:63]\n"
    "v_mfma_f32_16x16x32_bf16 v[56:59], v[80:83], v[120:123], v[56:59]\n"
    "v_mfma_f32_16x16x32_bf16 v[56:59], v[84:87], v[124:127], v[56:59]\n"
    "v_mfma_f32_16x16x32_bf16 v[52:55], v[72:75], v[120:123], v[52:55]\n"
    "v_mfma_f32_16x16x32_bf16 v[52:55], v[76:79], v[124:127], v[52:55]\n"
    "v_mfma_f32_16x16x32_bf16 v[48:51], v[64:67], v[120:123], v[48:51]\n"
    "v_mfma_f32_16x16x32_bf16 v[48:51], v[68:71], v[124:127], v[48:51]\n"
    "s_waitcnt lgkmcnt(0)\n"
    "s_barrier\n"
    "v_mfma_f32_16x16x32_bf16 v[0:3], v[128:131], v[160:163], v[0:3]\n"
    "ds_read_b128 v[96:99], v222 offset:32768\n"
    "v_mfma_f32_16x16x32_bf16 v[0:3], v[132:135], v[164:167], v[0:3]\n"
    "ds_read_b128 v[100:103], v223 offset:32768\n"
    "v_mfma_f32_16x16x32_bf16 v[4:7], v[136:139], v[160:163], v[4:7]\n"
    "ds_read_b128 v[64:67], v224 offset:32768\n"
    "v_mfma_f32_16x16x32_bf16 v[4:7], v[140:143], v[164:167], v[4:7]\n"
    "ds_read_b128 v[68:71], v224 offset:33792\n"
    "v_mfma_f32_16x16x32_bf16 v[8:11], v[144:147], v[160:163], v[8:11]\n"
    "ds_read_b128 v[72:75], v224 offset:34816\n"
    "v_mfma_f32_16x16x32_bf16 v[8:11], v[148:151], v[164:167], v[8:11]\n"
    "ds_read_b128 v[76:79], v224 offset:35840\n"
    "v_mfma_f32_16x16x32_bf16 v[12:15], v[152:155], v[160:163], v[12:15]\n"
    "ds_read_b128 v[80:83], v224 offset:36864\n"
    "v_mfma_f32_16x16x32_bf16 v[12:15], v[156:159], v[164:167], v[12:15]\n"
    "ds_read_b128 v[84:87], v224 offset:37888\n"
    "v_mfma_f32_16x16x32_bf16 v[28:31], v[152:155], v[168:171], v[28:31]\n"
    "ds_read_b128 v[88:91], v224 offset:38912\n"
    "v_mfma_f32_16x16x32_bf16 v[28:31], v[156:159], v[172:175], v[28:31]\n"
    "ds_read_b128 v[92:95], v224 offset:39936\n"
    "v_mfma_f32_16x16x32_bf16 v[24:27], v[144:147], v[168:171], v[24:27]\n"
    "ds_read_b128 v[104:107], v222 offset:34816\n"
    "v_mfma_f32_16x16x32_bf16 v[24:27], v[148:151], v[172:175], v[24:27]\n"
    "ds_read_b128 v[108:111], v223 offset:34816\n"
    "v_mfma_f32_16x16x32_bf16 v[20:23], v[136:139], v[168:171], v[20:23]\n"
    "ds_read_b128 v[112:115], v222 offset:36864\n"
    "v_mfma_f32_16x16x32_bf16 v[20:23], v[140:143], v[172:175], v[20:23]\n"
    "ds_read_b128 v[116:119], v223 offset:36864\n"
    "v_mfma_f32_16x16x32_bf16 v[16:19], v[128:131], v[168:171], v[16:19]\n"
    "ds_read_b128 v[120:123], v222 offset:38912\n"
    "v_mfma_f32_16x16x32_bf16 v[16:19], v[132:135], v[172:175], v[16:19]\n"
    "ds_read_b128 v[124:127], v223 offset:38912\n"
    "v_mfma_f32_16x16x32_bf16 v[32:35], v[128:131], v[176:179], v[32:35]\n"
    "v_mfma_f32_16x16x32_bf16 v[32:35], v[132:135], v[180:183], v[32:35]\n"
    "v_mfma_f32_16x16x32_bf16 v[36:39], v[136:139], v[176:179], v[36:39]\n"
    "v_mfma_f32_16x16x32_bf16 v[36:39], v[140:143], v[180:183], v[36:39]\n"
    "v_mfma_f32_16x16x32_bf16 v[40:43], v[144:147], v[176:179], v[40:43]\n"
    "v_mfma_f32_16x16x32_bf16 v[40:43], v[148:151], v[180:183], v[40:43]\n"
    "v_mfma_f32_16x16x32_bf16 v[44:47], v[152:155], v[176:179], v[44:47]\n"
    "v_mfma_f32_16x16x32_bf16 v[44:47], v[156:159], v[180:183], v[44:47]\n"
    "v_mfma_f32_16x16x32_bf16 v[60:63], v[152:155], v[184:187], v[60:63]\n"
    "v_mfma_f32_16x16x32_bf16 v[60:63], v[156:159], v[188:191], v[60:63]\n"
    "v_mfma_f32_16x16x32_bf16 v[56:59], v[144:147], v[184:187], v[56:59]\n"
    "v_mfma_f32_16x16x32_bf16 v[56:59], v[148:151], v[188:191], v[56:59]\n"
    "v_mfma_f32_16x16x32_bf16 v[52:55], v[136:139], v[184:187], v[52:55]\n"
    "v_mfma_f32_16x16x32_bf16 v[52:55], v[140:143], v[188:191], v[52:55]\n"
    "v_mfma_f32_16x16x32_bf16 v[48:51], v[128:131], v[184:187], v[48:51]\n"
    "v_mfma_f32_16x16x32_bf16 v[48:51], v[132:135], v[188:191], v[48:51]\n"
    "s_waitcnt lgkmcnt(0)\n"
    "s_barrier\n"
    "v_mfma_f32_16x16x32_bf16 v[0:3], v[64:67], v[96:99], v[0:3]\n"
    "ds_read_b128 v[160:163], v220 offset:32768\n"
    "v_mfma_f32_16x16x32_bf16 v[0:3], v[68:71], v[100:103], v[0:3]\n"
    "ds_read_b128 v[164:167], v221 offset:32768\n"
    "v_mfma_f32_16x16x32_bf16 v[4:7], v[72:75], v[96:99], v[4:7]\n"
    "ds_read_b128 v[128:131], v224 offset:49152\n"
    "v_mfma_f32_16x16x32_bf16 v[4:7], v[76:79], v[100:103], v[4:7]\n"
    "ds_read_b128 v[132:135], v224 offset:50176\n"
    "v_mfma_f32_16x16x32_bf16 v[8:11], v[80:83], v[96:99], v[8:11]\n"
    "ds_read_b128 v[136:139], v224 offset:51200\n"
    "v_mfma_f32_16x16x32_bf16 v[8:11], v[84:87], v[100:103], v[8:11]\n"
    "ds_read_b128 v[140:143], v224 offset:52224\n"
    "v_mfma_f32_16x16x32_bf16 v[12:15], v[88:91], v[96:99], v[12:15]\n"
    "ds_read_b128 v[144:147], v224 offset:53248\n"
    "v_mfma_f32_16x16x32_bf16 v[12:15], v[92:95], v[100:103], v[12:15]\n"
    "ds_read_b128 v[148:151], v224 offset:54272\n"
    "v_mfma_f32_16x16x32_bf16 v[28:31], v[88:91], v[104:107], v[28:31]\n"
    "ds_read_b128 v[152:155], v224 offset:55296\n"
    "v_mfma_f32_16x16x32_bf16 v[28:31], v[92:95], v[108:111], v[28:31]\n"
    "ds_read_b128 v[156:159], v224 offset:56320\n"
    "v_mfma_f32_16x16x32_bf16 v[24:27], v[80:83], v[104:107], v[24:27]\n"
    "ds_read_b128 v[168:171], v220 offset:34816\n"
    "v_mfma_f32_16x16x32_bf16 v[24:27], v[84:87], v[108:111], v[24:27]\n"
    "ds_read_b128 v[172:175], v221 offset:34816\n"
    "v_mfma_f32_16x16x32_bf16 v[20:23], v[72:75], v[104:107], v[20:23]\n"
    "ds_read_b128 v[176:179], v220 offset:36864\n"
    "v_mfma_f32_16x16x32_bf16 v[20:23], v[76:79], v[108:111], v[20:23]\n"
    "ds_read_b128 v[180:183], v221 offset:36864\n"
    "v_mfma_f32_16x16x32_bf16 v[16:19], v[64:67], v[104:107], v[16:19]\n"
    "ds_read_b128 v[184:187], v220 offset:38912\n"
    "v_mfma_f32_16x16x32_bf16 v[16:19], v[68:71], v[108:111], v[16:19]\n"
    "ds_read_b128 v[188:191], v221 offset:38912\n"
    "v_mfma_f32_16x16x32_bf16 v[32:35], v[64:67], v[112:115], v[32:35]\n"
    "v_mfma_f32_16x16x32_bf16 v[32:35], v[68:71], v[116:119], v[32:35]\n"
    "v_mfma_f32_16x16x32_bf16 v[36:39], v[72:75], v[112:115], v[36:39]\n"
    "v_mfma_f32_16x16x32_bf16 v[36:39], v[76:79], v[116:119], v[36:39]\n"
    "v_mfma_f32_16x16x32_bf16 v[40:43], v[80:83], v[112:115], v[40:43]\n"
    "v_mfma_f32_16x16x32_bf16 v[40:43], v[84:87], v[116:119], v[40:43]\n"
    "v_mfma_f32_16x16x32_bf16 v[44:47], v[88:91], v[112:115], v[44:47]\n"
    "v_mfma_f32_16x16x32_bf16 v[44:47], v[92:95], v[116:119], v[44:47]\n"
    "v_mfma_f32_16x16x32_bf16 v[60:63], v[88:91], v[120:123], v[60:63]\n"
    "v_mfma_f32_16x16x32_bf16 v[60:63], v[92:95], v[124:127], v[60:63]\n"
    "v_mfma_f32_16x16x32_bf16 v[56:59], v[80:83], v[120:123], v[56:59]\n"
    "v_mfma_f32_16x16x32_bf16 v[56:59], v[84:87], v[124:127], v[56:59]\n"
    "v_mfma_f32_16x16x32_bf16 v[52:55], v[72:75], v[120:123], v[52:55]\n"
    "v_mfma_f32_16x16x32_bf16 v[52:55], v[76:79], v[124:127], v[52:55]\n"
    "v_mfma_f32_16x16x32_bf16 v[48:51], v[64:67], v[120:123], v[48:51]\n"
    "v_mfma_f32_16x16x32_bf16 v[48:51], v[68:71], v[124:127], v[48:51]\n"
    "s_waitcnt lgkmcnt(0)\n"
    "s_barrier\n"
    "v_mfma_f32_16x16x32_bf16 v[0:3], v[128:131], v[160:163], v[0:3]\n"
    "ds_read_b128 v[96:99], v226 offset:0\n"
    "v_mfma_f32_16x16x32_bf16 v[0:3], v[132:135], v[164:167], v[0:3]\n"
    "ds_read_b128 v[100:103], v226 offset:1024\n"
    "v_mfma_f32_16x16x32_bf16 v[4:7], v[136:139], v[160:163], v[4:7]\n"
    "ds_read_b64_tr_b16 v[64:65], v228 offset:0\n"
    "v_mfma_f32_16x16x32_bf16 v[4:7], v[140:143], v[164:167], v[4:7]\n"
    "ds_read_b64_tr_b16 v[66:67], v228 offset:1024\n"
    "v_mfma_f32_16x16x32_bf16 v[8:11], v[144:147], v[160:163], v[8:11]\n"
    "ds_read_b64_tr_b16 v[68:69], v228 offset:8192\n"
    "v_mfma_f32_16x16x32_bf16 v[8:11], v[148:151], v[164:167], v[8:11]\n"
    "ds_read_b64_tr_b16 v[70:71], v228 offset:9216\n"
    "v_mfma_f32_16x16x32_bf16 v[12:15], v[152:155], v[160:163], v[12:15]\n"
    "ds_read_b64_tr_b16 v[72:73], v229 offset:0\n"
    "v_mul_f32_e64 v0, v248, v0\n"
    "v_mfma_f32_16x16x32_bf16 v[12:15], v[156:159], v[164:167], v[12:15]\n"
    "ds_read_b64_tr_b16 v[74:75], v229 offset:1024\n"
    "v_mul_f32_e64 v1, v248, v1\n"
    "v_mfma_f32_16x16x32_bf16 v[28:31], v[152:155], v[168:171], v[28:31]\n"
    "ds_read_b64_tr_b16 v[76:77], v229 offset:8192\n"
    "v_mul_f32_e64 v2, v248, v2\n"
    "v_mfma_f32_16x16x32_bf16 v[28:31], v[156:159], v[172:175], v[28:31]\n"
    "ds_read_b64_tr_b16 v[78:79], v229 offset:9216\n"
    "v_mul_f32_e64 v3, v248, v3\n"
    "v_mfma_f32_16x16x32_bf16 v[24:27], v[144:147], v[168:171], v[24:27]\n"
    "ds_read_b64_tr_b16 v[80:81], v230 offset:0\n"
    "v_mul_f32_e64 v4, v248, v4\n"
    "v_mfma_f32_16x16x32_bf16 v[24:27], v[148:151], v[172:175], v[24:27]\n"
    "ds_read_b64_tr_b16 v[82:83], v230 offset:1024\n"
    "v_mul_f32_e64 v5, v248, v5\n"
    "v_mfma_f32_16x16x32_bf16 v[20:23], v[136:139], v[168:171], v[20:23]\n"
    "ds_read_b64_tr_b16 v[84:85], v230 offset:8192\n"
    "v_mul_f32_e64 v6, v248, v6\n"
    "v_mfma_f32_16x16x32_bf16 v[20:23], v[140:143], v[172:175], v[20:23]\n"
    "ds_read_b64_tr_b16 v[86:87], v230 offset:9216\n"
    "v_mul_f32_e64 v7, v248, v7\n"
    "v_mfma_f32_16x16x32_bf16 v[16:19], v[128:131], v[168:171], v[16:19]\n"
    "ds_read_b64_tr_b16 v[88:89], v231 offset:0\n"
    "v_mul_f32_e64 v8, v248, v8\n"
    "v_mfma_f32_16x16x32_bf16 v[16:19], v[132:135], v[172:175], v[16:19]\n"
    "ds_read_b64_tr_b16 v[90:91], v231 offset:1024\n"
    "v_mul_f32_e64 v9, v248, v9\n"
    "v_mfma_f32_16x16x32_bf16 v[32:35], v[128:131], v[176:179], v[32:35]\n"
    "ds_read_b64_tr_b16 v[92:93], v231 offset:8192\n"
    "v_mul_f32_e64 v10, v248, v10\n"
    "v_mfma_f32_16x16x32_bf16 v[32:35], v[132:135], v[180:183], v[32:35]\n"
    "ds_read_b64_tr_b16 v[94:95], v231 offset:9216\n"
    "v_mul_f32_e64 v11, v248, v11\n"
    "v_mfma_f32_16x16x32_bf16 v[36:39], v[136:139], v[176:179], v[36:39]\n"
    "ds_read_b128 v[104:107], v226 offset:2048\n"
    "v_mul_f32_e64 v12, v248, v12\n"
    "v_mfma_f32_16x16x32_bf16 v[36:39], v[140:143], v[180:183], v[36:39]\n"
    "ds_read_b128 v[108:111], v226 offset:3072\n"
    "v_mul_f32_e64 v13, v248, v13\n"
    "v_mfma_f32_16x16x32_bf16 v[40:43], v[144:147], v[176:179], v[40:43]\n"
    "ds_read_b128 v[112:115], v226 offset:4096\n"
    "v_mul_f32_e64 v14, v248, v14\n"
    "v_mfma_f32_16x16x32_bf16 v[40:43], v[148:151], v[180:183], v[40:43]\n"
    "ds_read_b128 v[116:119], v226 offset:5120\n"
    "v_mul_f32_e64 v15, v248, v15\n"
    "v_mfma_f32_16x16x32_bf16 v[44:47], v[152:155], v[176:179], v[44:47]\n"
    "ds_read_b128 v[120:123], v226 offset:6144\n"
    "v_mul_f32_e64 v28, v249, v28\n"
    "v_mfma_f32_16x16x32_bf16 v[44:47], v[156:159], v[180:183], v[44:47]\n"
    "ds_read_b128 v[124:127], v226 offset:7168\n"
    "v_mul_f32_e64 v29, v249, v29\n"
    "v_mfma_f32_16x16x32_bf16 v[60:63], v[152:155], v[184:187], v[60:63]\n"
    "v_mul_f32_e64 v30, v249, v30\n"
    "v_mul_f32_e64 v31, v249, v31\n"
    "v_mfma_f32_16x16x32_bf16 v[60:63], v[156:159], v[188:191], v[60:63]\n"
    "v_mul_f32_e64 v24, v249, v24\n"
    "v_mul_f32_e64 v25, v249, v25\n"
    "v_mfma_f32_16x16x32_bf16 v[56:59], v[144:147], v[184:187], v[56:59]\n"
    "v_mul_f32_e64 v26, v249, v26\n"
    "v_mul_f32_e64 v27, v249, v27\n"
    "v_mfma_f32_16x16x32_bf16 v[56:59], v[148:151], v[188:191], v[56:59]\n"
    "v_mul_f32_e64 v20, v249, v20\n"
    "v_mul_f32_e64 v21, v249, v21\n"
    "v_mfma_f32_16x16x32_bf16 v[52:55], v[136:139], v[184:187], v[52:55]\n"
    "v_mul_f32_e64 v22, v249, v22\n"
    "v_mul_f32_e64 v23, v249, v23\n"
    "v_mfma_f32_16x16x32_bf16 v[52:55], v[140:143], v[188:191], v[52:55]\n"
    "v_mul_f32_e64 v16, v249, v16\n"
    "v_mul_f32_e64 v17, v249, v17\n"
    "v_mfma_f32_16x16x32_bf16 v[48:51], v[128:131], v[184:187], v[48:51]\n"
    "v_mul_f32_e64 v18, v249, v18\n"
    "v_mul_f32_e64 v19, v249, v19\n"
    "v_mfma_f32_16x16x32_bf16 v[48:51], v[132:135], v[188:191], v[48:51]\n"
    "v_mul_f32_e64 v32, v250, v32\n"
    "v_mul_f32_e64 v33, v250, v33\n"
    ".LPAD2_%=:\n"
    "s_cbranch_execz .LRET_%=\n"
    "s_nop 0\n"
    "s_waitcnt lgkmcnt(0)\n"
    "s_barrier\n"
    "v_mfma_f32_16x16x32_bf16 v[0:3], v[64:67], v[96:99], v[0:3]\n"
    "ds_read_b128 v[160:163], v226 offset:16384\n"
    "v_mul_f32_e64 v34, v250, v34\n"
    "v_mfma_f32_16x16x32_bf16 v[0:3], v[68:71], v[100:103], v[0:3]\n"
    "ds_read_b128 v[164:167], v226 offset:17408\n"
    "v_mul_f32_e64 v35, v250, v35\n"
    "v_mfma_f32_16x16x32_bf16 v[4:7], v[72:75], v[96:99], v[4:7]\n"
    "ds_read_b64_tr_b16 v[128:129], v228 offset:16384\n"
    "v_mul_f32_e64 v36, v250, v36\n"
    "v_mfma_f32_16x16x32_bf16 v[4:7], v[76:79], v[100:103], v[4:7]\n"
    "ds_read_b64_tr_b16 v[130:131], v228 offset:17408\n"
    "v_mul_f32_e64 v37, v250, v37\n"
    "v_mfma_f32_16x16x32_bf16 v[8:11], v[80:83], v[96:99], v[8:11]\n"
    "ds_read_b64_tr_b16 v[132:133], v228 offset:24576\n"
    "v_mul_f32_e64 v38, v250, v38\n"
    "v_mfma_f32_16x16x32_bf16 v[8:11], v[84:87], v[100:103], v[8:11]\n"
    "ds_read_b64_tr_b16 v[134:135], v228 offset:25600\n"
    "v_mul_f32_e64 v39, v250, v39\n"
    "v_mfma_f32_16x16x32_bf16 v[12:15], v[88:91], v[96:99], v[12:15]\n"
    "ds_read_b64_tr_b16 v[136:137], v229 offset:16384\n"
    "v_mul_f32_e64 v40, v250, v40\n"
    "v_mfma_f32_16x16x32_bf16 v[12:15], v[92:95], v[100:103], v[12:15]\n"
    "ds_read_b64_tr_b16 v[138:139], v229 offset:17408\n"
    "v_mul_f32_e64 v41, v250, v41\n"
    "v_mfma_f32_16x16x32_bf16 v[28:31], v[88:91], v[104:107], v[28:31]\n"
    "ds_read_b64_tr_b16 v[140:141], v229 offset:24576\n"
    "v_mul_f32_e64 v42, v250, v42\n"
    "v_mfma_f32_16x16x32_bf16 v[28:31], v[92:95], v[108:111], v[28:31]\n"
    "ds_read_b64_tr_b16 v[142:143], v229 offset:25600\n"
    "v_mul_f32_e64 v43, v250, v43\n"
    "v_mfma_f32_16x16x32_bf16 v[24:27], v[80:83], v[104:107], v[24:27]\n"
    "ds_read_b64_tr_b16 v[144:145], v230 offset:16384\n"
    "v_mul_f32_e64 v44, v250, v44\n"
    "v_mfma_f32_16x16x32_bf16 v[24:27], v[84:87], v[108:111], v[24:27]\n"
    "ds_read_b64_tr_b16 v[146:147], v230 offset:17408\n"
    "v_mul_f32_e64 v45, v250, v45\n"
    "v_mfma_f32_16x16x32_bf16 v[20:23], v[72:75], v[104:107], v[20:23]\n"
    "ds_read_b64_tr_b16 v[148:149], v230 offset:24576\n"
    "v_mul_f32_e64 v46, v250, v46\n"
    "v_mfma_f32_16x16x32_bf16 v[20:23], v[76:79], v[108:111], v[20:23]\n"
    "ds_read_b64_tr_b16 v[150:151], v230 offset:25600\n"
    "v_mul_f32_e64 v47, v250, v47\n"
    "v_mfma_f32_16x16x32_bf16 v[16:19], v[64:67], v[104:107], v[16:19]\n"
    "ds_read_b64_tr_b16 v[152:153], v231 offset:16384\n"
    "v_mul_f32_e64 v60, v251, v60\n"
    "v_mfma_f32_16x16x32_bf16 v[16:19], v[68:71], v[108:111], v[16:19]\n"
    "ds_read_b64_tr_b16 v[154:155], v231 offset:17408\n"
    "v_mul_f32_e64 v61, v251, v61\n"
    "v_mfma_f32_16x16x32_bf16 v[32:35], v[64:67], v[112:115], v[32:35]\n"
    "ds_read_b64_tr_b16 v[156:157], v231 offset:24576\n"
    "v_mul_f32_e64 v62, v251, v62\n"
    "v_mfma_f32_16x16x32_bf16 v[32:35], v[68:71], v[116:119], v[32:35]\n"
    "ds_read_b64_tr_b16 v[158:159], v231 offset:25600\n"
    "v_mul_f32_e64 v63, v251, v63\n"
    "v_mfma_f32_16x16x32_bf16 v[36:39], v[72:75], v[112:115], v[36:39]\n"
    "ds_read_b128 v[168:171], v226 offset:18432\n"
    "v_mul_f32_e64 v56, v251, v56\n"
    "v_mfma_f32_16x16x32_bf16 v[36:39], v[76:79], v[116:119], v[36:39]\n"
    "ds_read_b128 v[172:175], v226 offset:19456\n"
    "v_mul_f32_e64 v57, v251, v57\n"
    "v_mfma_f32_16x16x32_bf16 v[40:43], v[80:83], v[112:115], v[40:43]\n"
    "ds_read_b128 v[176:179], v226 offset:20480\n"
    "v_mul_f32_e64 v58, v251, v58\n"
    "v_mfma_f32_16x16x32_bf16 v[40:43], v[84:87], v[116:119], v[40:43]\n"
    "ds_read_b128 v[180:183], v226 offset:21504\n"
    "v_mul_f32_e64 v59, v251, v59\n"
    "v_mfma_f32_16x16x32_bf16 v[44:47], v[88:91], v[112:115], v[44:47]\n"
    "ds_read_b128 v[184:187], v226 offset:22528\n"
    "v_mul_f32_e64 v52, v251, v52\n"
    "v_mfma_f32_16x16x32_bf16 v[44:47], v[92:95], v[116:119], v[44:47]\n"
    "ds_read_b128 v[188:191], v226 offset:23552\n"
    "v_mul_f32_e64 v53, v251, v53\n"
    "v_mfma_f32_16x16x32_bf16 v[60:63], v[88:91], v[120:123], v[60:63]\n"
    "v_mul_f32_e64 v54, v251, v54\n"
    "v_mul_f32_e64 v55, v251, v55\n"
    "v_mfma_f32_16x16x32_bf16 v[60:63], v[92:95], v[124:127], v[60:63]\n"
    "v_mul_f32_e64 v48, v251, v48\n"
    "v_mul_f32_e64 v49, v251, v49\n"
    "v_mfma_f32_16x16x32_bf16 v[56:59], v[80:83], v[120:123], v[56:59]\n"
    "v_mul_f32_e64 v50, v251, v50\n"
    "v_mul_f32_e64 v51, v251, v51\n"
    "v_mfma_f32_16x16x32_bf16 v[56:59], v[84:87], v[124:127], v[56:59]\n"
    "v_mfma_f32_16x16x32_bf16 v[52:55], v[72:75], v[120:123], v[52:55]\n"
    "v_mfma_f32_16x16x32_bf16 v[52:55], v[76:79], v[124:127], v[52:55]\n"
    "v_mfma_f32_16x16x32_bf16 v[48:51], v[64:67], v[120:123], v[48:51]\n"
    "v_mfma_f32_16x16x32_bf16 v[48:51], v[68:71], v[124:127], v[48:51]\n"
    "s_waitcnt lgkmcnt(0)\n"
    "s_barrier\n"
    "v_mfma_f32_16x16x32_bf16 v[0:3], v[128:131], v[160:163], v[0:3]\n"
    "ds_read_b128 v[96:99], v227 offset:0\n"
    "v_mfma_f32_16x16x32_bf16 v[0:3], v[132:135], v[164:167], v[0:3]\n"
    "ds_read_b128 v[100:103], v227 offset:1024\n"
    "v_mfma_f32_16x16x32_bf16 v[4:7], v[136:139], v[160:163], v[4:7]\n"
    "ds_read_b64_tr_b16 v[64:65], v228 offset:32768\n"
    "v_mfma_f32_16x16x32_bf16 v[4:7], v[140:143], v[164:167], v[4:7]\n"
    "ds_read_b64_tr_b16 v[66:67], v228 offset:33792\n"
    "v_mfma_f32_16x16x32_bf16 v[8:11], v[144:147], v[160:163], v[8:11]\n"
    "ds_read_b64_tr_b16 v[68:69], v228 offset:40960\n"
    "v_mfma_f32_16x16x32_bf16 v[8:11], v[148:151], v[164:167], v[8:11]\n"
    "ds_read_b64_tr_b16 v[70:71], v228 offset:41984\n"
    "v_mfma_f32_16x16x32_bf16 v[12:15], v[152:155], v[160:163], v[12:15]\n"
    "ds_read_b64_tr_b16 v[72:73], v229 offset:32768\n"
    "v_mfma_f32_16x16x32_bf16 v[12:15], v[156:159], v[164:167], v[12:15]\n"
    "ds_read_b64_tr_b16 v[74:75], v229 offset:33792\n"
    "v_mfma_f32_16x16x32_bf16 v[28:31], v[152:155], v[168:171], v[28:31]\n"
    "ds_read_b64_tr_b16 v[76:77], v229 offset:40960\n"
    "v_mfma_f32_16x16x32_bf16 v[28:31], v[156:159], v[172:175], v[28:31]\n"
    "ds_read_b64_tr_b16 v[78:79], v229 offset:41984\n"
    "v_mfma_f32_16x16x32_bf16 v[24:27], v[144:147], v[168:171], v[24:27]\n"
    "ds_read_b64_tr_b16 v[80:81], v230 offset:32768\n"
    "v_mfma_f32_16x16x32_bf16 v[24:27], v[148:151], v[172:175], v[24:27]\n"
    "ds_read_b64_tr_b16 v[82:83], v230 offset:33792\n"
    "v_mfma_f32_16x16x32_bf16 v[20:23], v[136:139], v[168:171], v[20:23]\n"
    "ds_read_b64_tr_b16 v[84:85], v230 offset:40960\n"
    "v_mfma_f32_16x16x32_bf16 v[20:23], v[140:143], v[172:175], v[20:23]\n"
    "ds_read_b64_tr_b16 v[86:87], v230 offset:41984\n"
    "v_mfma_f32_16x16x32_bf16 v[16:19], v[128:131], v[168:171], v[16:19]\n"
    "ds_read_b64_tr_b16 v[88:89], v231 offset:32768\n"
    "v_mfma_f32_16x16x32_bf16 v[16:19], v[132:135], v[172:175], v[16:19]\n"
    "ds_read_b64_tr_b16 v[90:91], v231 offset:33792\n"
    "v_mfma_f32_16x16x32_bf16 v[32:35], v[128:131], v[176:179], v[32:35]\n"
    "ds_read_b64_tr_b16 v[92:93], v231 offset:40960\n"
    "v_mfma_f32_16x16x32_bf16 v[32:35], v[132:135], v[180:183], v[32:35]\n"
    "ds_read_b64_tr_b16 v[94:95], v231 offset:41984\n"
    "v_mfma_f32_16x16x32_bf16 v[36:39], v[136:139], v[176:179], v[36:39]\n"
    "ds_read_b128 v[104:107], v227 offset:2048\n"
    "v_mfma_f32_16x16x32_bf16 v[36:39], v[140:143], v[180:183], v[36:39]\n"
    "ds_read_b128 v[108:111], v227 offset:3072\n"
    "v_mfma_f32_16x16x32_bf16 v[40:43], v[144:147], v[176:179], v[40:43]\n"
    "ds_read_b128 v[112:115], v227 offset:4096\n"
    "v_mfma_f32_16x16x32_bf16 v[40:43], v[148:151], v[180:183], v[40:43]\n"
    "ds_read_b128 v[116:119], v227 offset:5120\n"
    "v_mfma_f32_16x16x32_bf16 v[44:47], v[152:155], v[176:179], v[44:47]\n"
    "ds_read_b128 v[120:123], v227 offset:6144\n"
    "v_mfma_f32_16x16x32_bf16 v[44:47], v[156:159], v[180:183], v[44:47]\n"
    "ds_read_b128 v[124:127], v227 offset:7168\n"
    "v_mfma_f32_16x16x32_bf16 v[60:63], v[152:155], v[184:187], v[60:63]\n"
    "v_mfma_f32_16x16x32_bf16 v[60:63], v[156:159], v[188:191], v[60:63]\n"
    "v_mfma_f32_16x16x32_bf16 v[56:59], v[144:147], v[184:187], v[56:59]\n"
    "v_mfma_f32_16x16x32_bf16 v[56:59], v[148:151], v[188:191], v[56:59]\n"
    "v_mfma_f32_16x16x32_bf16 v[52:55], v[136:139], v[184:187], v[52:55]\n"
    "v_mfma_f32_16x16x32_bf16 v[52:55], v[140:143], v[188:191], v[52:55]\n"
    "v_mfma_f32_16x16x32_bf16 v[48:51], v[128:131], v[184:187], v[48:51]\n"
    "v_mfma_f32_16x16x32_bf16 v[48:51], v[132:135], v[188:191], v[48:51]\n"
    "s_waitcnt lgkmcnt(0)\n"
    "s_barrier\n"
    "v_mfma_f32_16x16x32_bf16 v[0:3], v[64:67], v[96:99], v[0:3]\n"
    "ds_read_b128 v[160:163], v227 offset:16384\n"
    "v_mfma_f32_16x16x32_bf16 v[0:3], v[68:71], v[100:103], v[0:3]\n"
    "ds_read_b128 v[164:167], v227 offset:17408\n"
    "v_mfma_f32_16x16x32_bf16 v[4:7], v[72:75], v[96:99], v[4:7]\n"
    "ds_read_b64_tr_b16 v[128:129], v228 offset:49152\n"
    "v_mfma_f32_16x16x32_bf16 v[4:7], v[76:79], v[100:103], v[4:7]\n"
    "ds_read_b64_tr_b16 v[130:131], v228 offset:50176\n"
    "v_mfma_f32_16x16x32_bf16 v[8:11], v[80:83], v[96:99], v[8:11]\n"
    "ds_read_b64_tr_b16 v[132:133], v228 offset:57344\n"
    "v_mfma_f32_16x16x32_bf16 v[8:11], v[84:87], v[100:103], v[8:11]\n"
    "ds_read_b64_tr_b16 v[134:135], v228 offset:58368\n"
    "v_mfma_f32_16x16x32_bf16 v[12:15], v[88:91], v[96:99], v[12:15]\n"
    "ds_read_b64_tr_b16 v[136:137], v229 offset:49152\n"
    "v_mfma_f32_16x16x32_bf16 v[12:15], v[92:95], v[100:103], v[12:15]\n"
    "ds_read_b64_tr_b16 v[138:139], v229 offset:50176\n"
    "v_mfma_f32_16x16x32_bf16 v[28:31], v[88:91], v[104:107], v[28:31]\n"
    "ds_read_b64_tr_b16 v[140:141], v229 offset:57344\n"
    "v_mfma_f32_16x16x32_bf16 v[28:31], v[92:95], v[108:111], v[28:31]\n"
    "ds_read_b64_tr_b16 v[142:143], v229 offset:58368\n"
    "v_mfma_f32_16x16x32_bf16 v[24:27], v[80:83], v[104:107], v[24:27]\n"
    "ds_read_b64_tr_b16 v[144:145], v230 offset:49152\n"
    "v_mfma_f32_16x16x32_bf16 v[24:27], v[84:87], v[108:111], v[24:27]\n"
    "ds_read_b64_tr_b16 v[146:147], v230 offset:50176\n"
    "v_mfma_f32_16x16x32_bf16 v[20:23], v[72:75], v[104:107], v[20:23]\n"
    "ds_read_b64_tr_b16 v[148:149], v230 offset:57344\n"
    "v_mfma_f32_16x16x32_bf16 v[20:23], v[76:79], v[108:111], v[20:23]\n"
    "ds_read_b64_tr_b16 v[150:151], v230 offset:58368\n"
    "v_mfma_f32_16x16x32_bf16 v[16:19], v[64:67], v[104:107], v[16:19]\n"
    "ds_read_b64_tr_b16 v[152:153], v231 offset:49152\n"
    "v_mfma_f32_16x16x32_bf16 v[16:19], v[68:71], v[108:111], v[16:19]\n"
    "ds_read_b64_tr_b16 v[154:155], v231 offset:50176\n"
    "v_mfma_f32_16x16x32_bf16 v[32:35], v[64:67], v[112:115], v[32:35]\n"
    "ds_read_b64_tr_b16 v[156:157], v231 offset:57344\n"
    "v_mfma_f32_16x16x32_bf16 v[32:35], v[68:71], v[116:119], v[32:35]\n"
    "ds_read_b64_tr_b16 v[158:159], v231 offset:58368\n"
    "v_mfma_f32_16x16x32_bf16 v[36:39], v[72:75], v[112:115], v[36:39]\n"
    "ds_read_b128 v[168:171], v227 offset:18432\n"
    "v_mfma_f32_16x16x32_bf16 v[36:39], v[76:79], v[116:119], v[36:39]\n"
    "ds_read_b128 v[172:175], v227 offset:19456\n"
    "v_mfma_f32_16x16x32_bf16 v[40:43], v[80:83], v[112:115], v[40:43]\n"
    "ds_read_b128 v[176:179], v227 offset:20480\n"
    "v_mfma_f32_16x16x32_bf16 v[40:43], v[84:87], v[116:119], v[40:43]\n"
    "ds_read_b128 v[180:183], v227 offset:21504\n"
    "v_mfma_f32_16x16x32_bf16 v[44:47], v[88:91], v[112:115], v[44:47]\n"
    "ds_read_b128 v[184:187], v227 offset:22528\n"
    "v_mfma_f32_16x16x32_bf16 v[44:47], v[92:95], v[116:119], v[44:47]\n"
    "ds_read_b128 v[188:191], v227 offset:23552\n"
    "v_mfma_f32_16x16x32_bf16 v[60:63], v[88:91], v[120:123], v[60:63]\n"
    "v_mfma_f32_16x16x32_bf16 v[60:63], v[92:95], v[124:127], v[60:63]\n"
    "v_mfma_f32_16x16x32_bf16 v[56:59], v[80:83], v[120:123], v[56:59]\n"
    "v_mfma_f32_16x16x32_bf16 v[56:59], v[84:87], v[124:127], v[56:59]\n"
    "v_mfma_f32_16x16x32_bf16 v[52:55], v[72:75], v[120:123], v[52:55]\n"
    "v_mfma_f32_16x16x32_bf16 v[52:55], v[76:79], v[124:127], v[52:55]\n"
    "v_mfma_f32_16x16x32_bf16 v[48:51], v[64:67], v[120:123], v[48:51]\n"
    "v_mfma_f32_16x16x32_bf16 v[48:51], v[68:71], v[124:127], v[48:51]\n"
    "s_waitcnt lgkmcnt(0)\n"
    "s_barrier\n"
    "v_mfma_f32_16x16x32_bf16 v[0:3], v[128:131], v[160:163], v[0:3]\n"
    "ds_read_b128 v[96:99], v227 offset:32768\n"
    "v_mfma_f32_16x16x32_bf16 v[0:3], v[132:135], v[164:167], v[0:3]\n"
    "ds_read_b128 v[100:103], v227 offset:33792\n"
    "v_mfma_f32_16x16x32_bf16 v[4:7], v[136:139], v[160:163], v[4:7]\n"
    "ds_read_b64_tr_b16 v[64:65], v228 offset:0\n"
    "v_mfma_f32_16x16x32_bf16 v[4:7], v[140:143], v[164:167], v[4:7]\n"
    "ds_read_b64_tr_b16 v[66:67], v228 offset:1024\n"
    "v_mfma_f32_16x16x32_bf16 v[8:11], v[144:147], v[160:163], v[8:11]\n"
    "ds_read_b64_tr_b16 v[68:69], v228 offset:8192\n"
    "v_mfma_f32_16x16x32_bf16 v[8:11], v[148:151], v[164:167], v[8:11]\n"
    "ds_read_b64_tr_b16 v[70:71], v228 offset:9216\n"
    "v_mfma_f32_16x16x32_bf16 v[12:15], v[152:155], v[160:163], v[12:15]\n"
    "ds_read_b64_tr_b16 v[72:73], v229 offset:0\n"
    "v_mfma_f32_16x16x32_bf16 v[12:15], v[156:159], v[164:167], v[12:15]\n"
    "ds_read_b64_tr_b16 v[74:75], v229 offset:1024\n"
    "v_mfma_f32_16x16x32_bf16 v[28:31], v[152:155], v[168:171], v[28:31]\n"
    "ds_read_b64_tr_b16 v[76:77], v229 offset:8192\n"
    "v_mfma_f32_16x16x32_bf16 v[28:31], v[156:159], v[172:175], v[28:31]\n"
    "ds_read_b64_tr_b16 v[78:79], v229 offset:9216\n"
    "v_mfma_f32_16x16x32_bf16 v[24:27], v[144:147], v[168:171], v[24:27]\n"
    "ds_read_b64_tr_b16 v[80:81], v230 offset:0\n"
    "v_mfma_f32_16x16x32_bf16 v[24:27], v[148:151], v[172:175], v[24:27]\n"
    "ds_read_b64_tr_b16 v[82:83], v230 offset:1024\n"
    "v_mfma_f32_16x16x32_bf16 v[20:23], v[136:139], v[168:171], v[20:23]\n"
    "ds_read_b64_tr_b16 v[84:85], v230 offset:8192\n"
    "v_mfma_f32_16x16x32_bf16 v[20:23], v[140:143], v[172:175], v[20:23]\n"
    "ds_read_b64_tr_b16 v[86:87], v230 offset:9216\n"
    "v_mfma_f32_16x16x32_bf16 v[16:19], v[128:131], v[168:171], v[16:19]\n"
    "ds_read_b64_tr_b16 v[88:89], v231 offset:0\n"
    "v_mfma_f32_16x16x32_bf16 v[16:19], v[132:135], v[172:175], v[16:19]\n"
    "ds_read_b64_tr_b16 v[90:91], v231 offset:1024\n"
    "v_mfma_f32_16x16x32_bf16 v[32:35], v[128:131], v[176:179], v[32:35]\n"
    "ds_read_b64_tr_b16 v[92:93], v231 offset:8192\n"
    "v_mfma_f32_16x16x32_bf16 v[32:35], v[132:135], v[180:183], v[32:35]\n"
    "ds_read_b64_tr_b16 v[94:95], v231 offset:9216\n"
    "v_mfma_f32_16x16x32_bf16 v[36:39], v[136:139], v[176:179], v[36:39]\n"
    "ds_read_b128 v[104:107], v227 offset:34816\n"
    "v_mfma_f32_16x16x32_bf16 v[36:39], v[140:143], v[180:183], v[36:39]\n"
    "ds_read_b128 v[108:111], v227 offset:35840\n"
    "v_mfma_f32_16x16x32_bf16 v[40:43], v[144:147], v[176:179], v[40:43]\n"
    "ds_read_b128 v[112:115], v227 offset:36864\n"
    "v_mfma_f32_16x16x32_bf16 v[40:43], v[148:151], v[180:183], v[40:43]\n"
    "ds_read_b128 v[116:119], v227 offset:37888\n"
    "v_mfma_f32_16x16x32_bf16 v[44:47], v[152:155], v[176:179], v[44:47]\n"
    "ds_read_b128 v[120:123], v227 offset:38912\n"
    "v_mfma_f32_16x16x32_bf16 v[44:47], v[156:159], v[180:183], v[44:47]\n"
    "ds_read_b128 v[124:127], v227 offset:39936\n"
    "v_mfma_f32_16x16x32_bf16 v[60:63], v[152:155], v[184:187], v[60:63]\n"
    "v_mfma_f32_16x16x32_bf16 v[60:63], v[156:159], v[188:191], v[60:63]\n"
    "v_mfma_f32_16x16x32_bf16 v[56:59], v[144:147], v[184:187], v[56:59]\n"
    "v_mfma_f32_16x16x32_bf16 v[56:59], v[148:151], v[188:191], v[56:59]\n"
    "v_mfma_f32_16x16x32_bf16 v[52:55], v[136:139], v[184:187], v[52:55]\n"
    "v_mfma_f32_16x16x32_bf16 v[52:55], v[140:143], v[188:191], v[52:55]\n"
    "v_mfma_f32_16x16x32_bf16 v[48:51], v[128:131], v[184:187], v[48:51]\n"
    "v_mfma_f32_16x16x32_bf16 v[48:51], v[132:135], v[188:191], v[48:51]\n"
    "s_waitcnt lgkmcnt(0)\n"
    "s_barrier\n"
    "v_mfma_f32_16x16x32_bf16 v[16:19], v[64:67], v[104:107], v[16:19]\n"
    "ds_read_b128 v[204:207], v226 offset:34816\n"
    "ds_read_b128 v[208:211], v226 offset:35840\n"
    "v_mfma_f32_16x16x32_bf16 v[16:19], v[68:71], v[108:111], v[16:19]\n"
    "ds_read_b64_tr_b16 v[128:129], v228 offset:16384\n"
    "ds_read_b64_tr_b16 v[130:131], v228 offset:17408\n"
    "v_mfma_f32_16x16x32_bf16 v[20:23], v[72:75], v[104:107], v[20:23]\n"
    "ds_read_b64_tr_b16 v[132:133], v228 offset:24576\n"
    "ds_read_b64_tr_b16 v[134:135], v228 offset:25600\n"
    "v_mfma_f32_16x16x32_bf16 v[20:23], v[76:79], v[108:111], v[20:23]\n"
    "ds_read_b64_tr_b16 v[136:137], v229 offset:16384\n"
    "ds_read_b64_tr_b16 v[138:139], v229 offset:17408\n"
    "v_mfma_f32_16x16x32_bf16 v[24:27], v[80:83], v[104:107], v[24:27]\n"
    "ds_read_b64_tr_b16 v[140:141], v229 offset:24576\n"
    "ds_read_b64_tr_b16 v[142:143], v229 offset:25600\n"
    "v_mfma_f32_16x16x32_bf16 v[24:27], v[84:87], v[108:111], v[24:27]\n"
    "ds_read_b64_tr_b16 v[144:145], v230 offset:16384\n"
    "ds_read_b64_tr_b16 v[146:147], v230 offset:17408\n"
    "v_mfma_f32_16x16x32_bf16 v[28:31], v[88:91], v[104:107], v[28:31]\n"
    "ds_read_b64_tr_b16 v[148:149], v230 offset:24576\n"
    "ds_read_b64_tr_b16 v[150:151], v230 offset:25600\n"
    "v_mfma_f32_16x16x32_bf16 v[28:31], v[92:95], v[108:111], v[28:31]\n"
    "ds_read_b64_tr_b16 v[152:153], v231 offset:16384\n"
    "ds_read_b64_tr_b16 v[154:155], v231 offset:17408\n"
    "v_mfma_f32_16x16x32_bf16 v[32:35], v[64:67], v[112:115], v[32:35]\n"
    "ds_read_b64_tr_b16 v[156:157], v231 offset:24576\n"
    "ds_read_b64_tr_b16 v[158:159], v231 offset:25600\n"
    "v_mfma_f32_16x16x32_bf16 v[32:35], v[68:71], v[116:119], v[32:35]\n"
    "v_mfma_f32_16x16x32_bf16 v[36:39], v[72:75], v[112:115], v[36:39]\n"
    "v_mfma_f32_16x16x32_bf16 v[36:39], v[76:79], v[116:119], v[36:39]\n"
    "v_mfma_f32_16x16x32_bf16 v[40:43], v[80:83], v[112:115], v[40:43]\n"
    "v_mfma_f32_16x16x32_bf16 v[40:43], v[84:87], v[116:119], v[40:43]\n"
    "s_barrier\n"
    "s_nop 0\n"
    "v_mfma_f32_16x16x32_bf16 v[44:47], v[88:91], v[112:115], v[44:47]\n"
    "ds_read_b128 v[212:215], v227 offset:2048\n"
    "ds_read_b128 v[216:219], v227 offset:3072\n"
    "v_mfma_f32_16x16x32_bf16 v[44:47], v[92:95], v[116:119], v[44:47]\n"
    "ds_read_b64_tr_b16 v[160:161], v228 offset:32768\n"
    "ds_read_b64_tr_b16 v[162:163], v228 offset:33792\n"
    "s_waitcnt lgkmcnt(14)\n"
    "v_mfma_f32_16x16x32_bf16 v[16:19], v[128:131], v[204:207], v[16:19]\n"
    "ds_read_b64_tr_b16 v[164:165], v228 offset:40960\n"
    "ds_read_b64_tr_b16 v[166:167], v228 offset:41984\n"
    "v_mfma_f32_16x16x32_bf16 v[16:19], v[132:135], v[208:211], v[16:19]\n"
    "ds_read_b64_tr_b16 v[168:169], v229 offset:32768\n"
    "ds_read_b64_tr_b16 v[170:171], v229 offset:33792\n"
    "s_waitcnt lgkmcnt(14)\n"
    "v_mfma_f32_16x16x32_bf16 v[20:23], v[136:139], v[204:207], v[20:23]\n"
    "ds_read_b64_tr_b16 v[172:173], v229 offset:40960\n"
    "ds_read_b64_tr_b16 v[174:175], v229 offset:41984\n"
    "v_mfma_f32_16x16x32_bf16 v[20:23], v[140:143], v[208:211], v[20:23]\n"
    "ds_read_b64_tr_b16 v[176:177], v230 offset:32768\n"
    "ds_read_b64_tr_b16 v[178:179], v230 offset:33792\n"
    "s_waitcnt lgkmcnt(14)\n"
    "v_mfma_f32_16x16x32_bf16 v[24:27], v[144:147], v[204:207], v[24:27]\n"
    "ds_read_b64_tr_b16 v[180:181], v230 offset:40960\n"
    "ds_read_b64_tr_b16 v[182:183], v230 offset:41984\n"
    "v_mfma_f32_16x16x32_bf16 v[24:27], v[148:151], v[208:211], v[24:27]\n"
    "ds_read_b64_tr_b16 v[184:185], v231 offset:32768\n"
    "ds_read_b64_tr_b16 v[186:187], v231 offset:33792\n"
    "s_waitcnt lgkmcnt(14)\n"
    "v_mfma_f32_16x16x32_bf16 v[28:31], v[152:155], v[204:207], v[28:31]\n"
    "ds_read_b64_tr_b16 v[188:189], v231 offset:40960\n"
    "ds_read_b64_tr_b16 v[190:191], v231 offset:41984\n"
    "v_mfma_f32_16x16x32_bf16 v[28:31], v[156:159], v[208:211], v[28:31]\n"
    "ds_read_b128 v[104:107], v226 offset:36864\n"
    "ds_read_b128 v[108:111], v226 offset:37888\n"
    "s_waitcnt lgkmcnt(14)\n"
    "v_mfma_f32_16x16x32_bf16 v[16:19], v[160:163], v[212:215], v[16:19]\n"
    "ds_read_b128 v[112:115], v227 offset:4096\n"
    "v_mfma_f32_16x16x32_bf16 v[16:19], v[164:167], v[216:219], v[16:19]\n"
    "ds_read_b128 v[116:119], v227 offset:5120\n"
    "s_waitcnt lgkmcnt(12)\n"
    "v_mfma_f32_16x16x32_bf16 v[20:23], v[168:171], v[212:215], v[20:23]\n"
    "v_mfma_f32_16x16x32_bf16 v[20:23], v[172:175], v[216:219], v[20:23]\n"
    "s_waitcnt lgkmcnt(8)\n"
    "v_mfma_f32_16x16x32_bf16 v[24:27], v[176:179], v[212:215], v[24:27]\n"
    "v_mfma_f32_16x16x32_bf16 v[24:27], v[180:183], v[216:219], v[24:27]\n"
    "s_waitcnt lgkmcnt(4)\n"
    "v_mfma_f32_16x16x32_bf16 v[28:31], v[184:187], v[212:215], v[28:31]\n"
    "v_mfma_f32_16x16x32_bf16 v[28:31], v[188:191], v[216:219], v[28:31]\n"
    "s_waitcnt lgkmcnt(2)\n"
    "v_mfma_f32_16x16x32_bf16 v[32:35], v[128:131], v[104:107], v[32:35]\n"
    "ds_read_b128 v[204:207], v226 offset:38912\n"
    "v_mfma_f32_16x16x32_bf16 v[32:35], v[132:135], v[108:111], v[32:35]\n"
    "ds_read_b128 v[208:211], v226 offset:39936\n"
    "v_cvt_pk_bf16_f32 v16, v16, v17\n"
    "v_cvt_pk_bf16_f32 v17, v18, v19\n"
    "s_waitcnt lgkmcnt(2)\n"
    "v_mfma_f32_16x16x32_bf16 v[32:35], v[160:163], v[112:115], v[32:35]\n"
    "ds_read_b128 v[212:215], v227 offset:6144\n"
    "v_cvt_pk_bf16_f32 v18, v20, v21\n"
    "v_cvt_pk_bf16_f32 v19, v22, v23\n"
    "v_mfma_f32_16x16x32_bf16 v[32:35], v[164:167], v[116:119], v[32:35]\n"
    "ds_read_b128 v[216:219], v227 offset:7168\n"
    "v_permlane16_swap_b32 v16, v18\n"
    "v_permlane16_swap_b32 v17, v19\n"
    "v_mfma_f32_16x16x32_bf16 v[36:39], v[136:139], v[104:107], v[36:39]\n"
    "buffer_store_dwordx4 v[16:19], v246, s[60:63], s8 offen offset:0 sc1\n"
    "v_mfma_f32_16x16x32_bf16 v[36:39], v[140:143], v[108:111], v[36:39]\n"
    "v_cvt_pk_bf16_f32 v24, v24, v25\n"
    "v_cvt_pk_bf16_f32 v25, v26, v27\n"
    "v_mfma_f32_16x16x32_bf16 v[36:39], v[168:171], v[112:115], v[36:39]\n"
    "v_cvt_pk_bf16_f32 v26, v28, v29\n"
    "v_cvt_pk_bf16_f32 v27, v30, v31\n"
    "v_mfma_f32_16x16x32_bf16 v[36:39], v[172:175], v[116:119], v[36:39]\n"
    "v_permlane16_swap_b32 v24, v26\n"
    "v_permlane16_swap_b32 v25, v27\n"
    "v_mfma_f32_16x16x32_bf16 v[40:43], v[144:147], v[104:107], v[40:43]\n"
    "buffer_store_dwordx4 v[24:27], v246, s[60:63], s8 offen offset:1024 sc1\n"
    "v_mfma_f32_16x16x32_bf16 v[40:43], v[148:151], v[108:111], v[40:43]\n"
    "v_mfma_f32_16x16x32_bf16 v[40:43], v[176:179], v[112:115], v[40:43]\n"
    "v_mfma_f32_16x16x32_bf16 v[40:43], v[180:183], v[116:119], v[40:43]\n"
    "v_mfma_f32_16x16x32_bf16 v[44:47], v[152:155], v[104:107], v[44:47]\n"
    "v_mfma_f32_16x16x32_bf16 v[44:47], v[156:159], v[108:111], v[44:47]\n"
    "v_cvt_pk_bf16_f32 v32, v32, v33\n"
    "v_cvt_pk_bf16_f32 v33, v34, v35\n"
    "v_mfma_f32_16x16x32_bf16 v[44:47], v[184:187], v[112:115], v[44:47]\n"
    "v_cvt_pk_bf16_f32 v34, v36, v37\n"
    "v_cvt_pk_bf16_f32 v35, v38, v39\n"
    "v_mfma_f32_16x16x32_bf16 v[44:47], v[188:191], v[116:119], v[44:47]\n"
    "v_permlane16_swap_b32 v32, v34\n"
    "v_permlane16_swap_b32 v33, v35\n"
    "v_mfma_f32_16x16x32_bf16 v[48:51], v[64:67], v[120:123], v[48:51]\n"
    "ds_read_b128 v[104:107], v226 offset:32768\n"
    "buffer_store_dwordx4 v[32:35], v246, s[60:63], s9 offen offset:0 sc1\n"
    "v_mfma_f32_16x16x32_bf16 v[48:51], v[68:71], v[124:127], v[48:51]\n"
    "ds_read_b128 v[108:111], v226 offset:33792\n"
    "s_waitcnt lgkmcnt(4)\n"
    "v_mfma_f32_16x16x32_bf16 v[48:51], v[128:131], v[204:207], v[48:51]\n"
    "ds_read_b128 v[112:115], v227 offset:0\n"
    "v_mfma_f32_16x16x32_bf16 v[48:51], v[132:135], v[208:211], v[48:51]\n"
    "ds_read_b128 v[116:119], v227 offset:1024\n"
    "s_waitcnt lgkmcnt(4)\n"
    "v_mfma_f32_16x16x32_bf16 v[48:51], v[160:163], v[212:215], v[48:51]\n"
    "v_mfma_f32_16x16x32_bf16 v[48:51], v[164:167], v[216:219], v[48:51]\n"
    "v_cvt_pk_bf16_f32 v40, v40, v41\n"
    "v_cvt_pk_bf16_f32 v41, v42, v43\n"
    "v_mfma_f32_16x16x32_bf16 v[52:55], v[72:75], v[120:123], v[52:55]\n"
    "v_cvt_pk_bf16_f32 v42, v44, v45\n"
    "v_cvt_pk_bf16_f32 v43, v46, v47\n"
    "v_mfma_f32_16x16x32_bf16 v[52:55], v[76:79], v[124:127], v[52:55]\n"
    "v_permlane16_swap_b32 v40, v42\n"
    "v_permlane16_swap_b32 v41, v43\n"
    "v_mfma_f32_16x16x32_bf16 v[52:55], v[136:139], v[204:207], v[52:55]\n"
    "buffer_store_dwordx4 v[40:43], v246, s[60:63], s9 offen offset:1024 sc1\n"
    "v_mfma_f32_16x16x32_bf16 v[52:55], v[140:143], v[208:211], v[52:55]\n"
    "v_mfma_f32_16x16x32_bf16 v[52:55], v[168:171], v[212:215], v[52:55]\n"
    "v_mfma_f32_16x16x32_bf16 v[52:55], v[172:175], v[216:219], v[52:55]\n"
    "v_mfma_f32_16x16x32_bf16 v[56:59], v[80:83], v[120:123], v[56:59]\n"
    "v_mfma_f32_16x16x32_bf16 v[56:59], v[84:87], v[124:127], v[56:59]\n"
    "v_mfma_f32_16x16x32_bf16 v[56:59], v[144:147], v[204:207], v[56:59]\n"
    "v_mfma_f32_16x16x32_bf16 v[56:59], v[148:151], v[208:211], v[56:59]\n"
    "v_mfma_f32_16x16x32_bf16 v[56:59], v[176:179], v[212:215], v[56:59]\n"
    "v_mfma_f32_16x16x32_bf16 v[56:59], v[180:183], v[216:219], v[56:59]\n"
    "v_cvt_pk_bf16_f32 v48, v48, v49\n"
    "v_cvt_pk_bf16_f32 v49, v50, v51\n"
    "v_mfma_f32_16x16x32_bf16 v[60:63], v[88:91], v[120:123], v[60:63]\n"
    "v_cvt_pk_bf16_f32 v50, v52, v53\n"
    "v_cvt_pk_bf16_f32 v51, v54, v55\n"
    "v_mfma_f32_16x16x32_bf16 v[60:63], v[92:95], v[124:127], v[60:63]\n"
    "v_permlane16_swap_b32 v48, v50\n"
    "v_permlane16_swap_b32 v49, v51\n"
    "v_mfma_f32_16x16x32_bf16 v[60:63], v[152:155], v[204:207], v[60:63]\n"
    "buffer_store_dwordx4 v[48:51], v246, s[60:63], s10 offen offset:0 sc1\n"
    "v_mfma_f32_16x16x32_bf16 v[60:63], v[156:159], v[208:211], v[60:63]\n"
    "v_mfma_f32_16x16x32_bf16 v[60:63], v[184:187], v[212:215], v[60:63]\n"
    "v_mfma_f32_16x16x32_bf16 v[60:63], v[188:191], v[216:219], v[60:63]\n"
    "v_mfma_f32_16x16x32_bf16 v[0:3], v[64:67], v[96:99], v[0:3]\n"
    "v_mfma_f32_16x16x32_bf16 v[0:3], v[68:71], v[100:103], v[0:3]\n"
    "s_waitcnt lgkmcnt(2)\n"
    "v_mfma_f32_16x16x32_bf16 v[0:3], v[128:131], v[104:107], v[0:3]\n"
    "v_mfma_f32_16x16x32_bf16 v[0:3], v[132:135], v[108:111], v[0:3]\n"
    "s_waitcnt lgkmcnt(0)\n"
    "v_mfma_f32_16x16x32_bf16 v[0:3], v[160:163], v[112:115], v[0:3]\n"
    "v_mfma_f32_16x16x32_bf16 v[0:3], v[164:167], v[116:119], v[0:3]\n"
    "v_cvt_pk_bf16_f32 v56, v56, v57\n"
    "v_cvt_pk_bf16_f32 v57, v58, v59\n"
    "v_mfma_f32_16x16x32_bf16 v[4:7], v[72:75], v[96:99], v[4:7]\n"
    "v_cvt_pk_bf16_f32 v58, v60, v61\n"
    "v_cvt_pk_bf16_f32 v59, v62, v63\n"
    "v_mfma_f32_16x16x32_bf16 v[4:7], v[76:79], v[100:103], v[4:7]\n"
    "v_permlane16_swap_b32 v56, v58\n"
    "v_permlane16_swap_b32 v57, v59\n"
    "v_mfma_f32_16x16x32_bf16 v[4:7], v[136:139], v[104:107], v[4:7]\n"
    "buffer_store_dwordx4 v[56:59], v246, s[60:63], s10 offen offset:1024 sc1\n"
    "v_mfma_f32_16x16x32_bf16 v[4:7], v[140:143], v[108:111], v[4:7]\n"
    "v_mfma_f32_16x16x32_bf16 v[4:7], v[168:171], v[112:115], v[4:7]\n"
    "v_mfma_f32_16x16x32_bf16 v[4:7], v[172:175], v[116:119], v[4:7]\n"
    "v_mfma_f32_16x16x32_bf16 v[8:11], v[80:83], v[96:99], v[8:11]\n"
    "v_mfma_f32_16x16x32_bf16 v[8:11], v[84:87], v[100:103], v[8:11]\n"
    "v_mfma_f32_16x16x32_bf16 v[8:11], v[144:147], v[104:107], v[8:11]\n"
    "v_mfma_f32_16x16x32_bf16 v[8:11], v[148:151], v[108:111], v[8:11]\n"
    "v_mfma_f32_16x16x32_bf16 v[8:11], v[176:179], v[112:115], v[8:11]\n"
    "v_mfma_f32_16x16x32_bf16 v[8:11], v[180:183], v[116:119], v[8:11]\n"
    "v_mfma_f32_16x16x32_bf16 v[12:15], v[88:91], v[96:99], v[12:15]\n"
    "v_mfma_f32_16x16x32_bf16 v[12:15], v[92:95], v[100:103], v[12:15]\n"
    "v_mfma_f32_16x16x32_bf16 v[12:15], v[152:155], v[104:107], v[12:15]\n"
    "v_mfma_f32_16x16x32_bf16 v[12:15], v[156:159], v[108:111], v[12:15]\n"
    "v_mfma_f32_16x16x32_bf16 v[12:15], v[184:187], v[112:115], v[12:15]\n"
    "v_mfma_f32_16x16x32_bf16 v[12:15], v[188:191], v[116:119], v[12:15]\n"
    "s_mov_b64 exec, 2\n"
    "s_waitcnt vmcnt(4)\n"
    "buffer_store_dwordx2 v[200:201], v232, s[60:63], 0 offen sc1\n"
    "s_mov_b64 exec, 4\n"
    "s_waitcnt vmcnt(3)\n"
    "buffer_store_dwordx2 v[200:201], v232, s[60:63], 0 offen sc1\n"
    "s_mov_b64 exec, 8\n"
    "s_waitcnt vmcnt(2)\n"
    "buffer_store_dwordx2 v[200:201], v232, s[60:63], 0 offen sc1\n"
    "s_barrier\n"
    "s_mov_b64 exec, -1\n"
    "ds_write_b128 v244, v[0:3] offset:0\n"
    "ds_write_b128 v245, v[4:7] offset:1024\n"
    "ds_write_b128 v244, v[8:11] offset:2048\n"
    "ds_write_b128 v245, v[12:15] offset:3072\n"
    "s_waitcnt lgkmcnt(0)\n"
    "s_barrier\n"
    "ds_read_b128 v[72:75], v233 offset:8192\n"
    "ds_read_b128 v[64:67], v225\n"
    "ds_read_b128 v[68:71], v225 offset:256\n"
    "ds_read_b128 v[76:79], v233 offset:16384\n"
    "ds_read_b128 v[80:83], v233 offset:24576\n"
    "s_waitcnt lgkmcnt(4)\n"
    "v_lshlrev_b32 v84, 16, v72\n"
    "v_and_b32 v85, 0xffff0000, v72\n"
    "v_lshlrev_b32 v86, 16, v73\n"
    "v_and_b32 v87, 0xffff0000, v73\n"
    "v_lshlrev_b32 v88, 16, v74\n"
    "v_and_b32 v89, 0xffff0000, v74\n"
    "v_lshlrev_b32 v90, 16, v75\n"
    "v_and_b32 v91, 0xffff0000, v75\n"
    "s_waitcnt lgkmcnt(2)\n"
    "v_pk_add_f32 v[64:65], v[64:65], v[84:85]\n"
    "v_pk_add_f32 v[66:67], v[66:67], v[86:87]\n"
    "v_pk_add_f32 v[68:69], v[68:69], v[88:89]\n"
    "v_pk_add_f32 v[70:71], v[70:71], v[90:91]\n"
    "s_waitcnt lgkmcnt(1)\n"
    "v_lshlrev_b32 v84, 16, v76\n"
    "v_and_b32 v85, 0xffff0000, v76\n"
    "v_lshlrev_b32 v86, 16, v77\n"
    "v_and_b32 v87, 0xffff0000, v77\n"
    "v_lshlrev_b32 v88, 16, v78\n"
    "v_and_b32 v89, 0xffff0000, v78\n"
    "v_lshlrev_b32 v90, 16, v79\n"
    "v_and_b32 v91, 0xffff0000, v79\n"
    "v_pk_add_f32 v[64:65], v[64:65], v[84:85]\n"
    "v_pk_add_f32 v[66:67], v[66:67], v[86:87]\n"
    "v_pk_add_f32 v[68:69], v[68:69], v[88:89]\n"
    "v_pk_add_f32 v[70:71], v[70:71], v[90:91]\n"
    "s_waitcnt lgkmcnt(0)\n"
    "v_lshlrev_b32 v84, 16, v80\n"
    "v_and_b32 v85, 0xffff0000, v80\n"
    "v_lshlrev_b32 v86, 16, v81\n"
    "v_and_b32 v87, 0xffff0000, v81\n"
    "v_lshlrev_b32 v88, 16, v82\n"
    "v_and_b32 v89, 0xffff0000, v82\n"
    "v_lshlrev_b32 v90, 16, v83\n"
    "v_and_b32 v91, 0xffff0000, v83\n"
    "v_pk_add_f32 v[64:65], v[64:65], v[84:85]\n"
    "v_pk_add_f32 v[66:67], v[66:67], v[86:87]\n"
    "v_pk_add_f32 v[68:69], v[68:69], v[88:89]\n"
    "v_pk_add_f32 v[70:71], v[70:71], v[90:91]\n"
    "v_cvt_pk_bf16_f32 v64, v64, v65\n"
    "v_cvt_pk_bf16_f32 v65, v66, v67\n"
    "v_cvt_pk_bf16_f32 v66, v68, v69\n"
    "v_cvt_pk_bf16_f32 v67, v70, v71\n"
    "buffer_store_dwordx4 v[64:67], v234, s[64:67], 0 offen sc1\n"
    "s_branch .LEND_%=\n"
    ".fill 19, 4, 0xbf800000\n"
    ".LAX_%=:\n"
    "v_lshrrev_b32 v241, 4, v240\n"
    "v_and_b32 v242, 15, v240\n"
    "v_lshlrev_b32 v247, 4, v240\n"
    "v_lshrrev_b32 v243, 1, v242\n"
    "v_xor_b32 v243, v243, v241\n"
    "v_lshlrev_b32 v243, 4, v243\n"
    "v_lshl_add_u32 v243, v242, 7, v243\n"
    "s_lshl_b32 s71, s75, 13\n"
    "v_add_u32 v232, s71, v243\n"
    "v_xor_b32 v233, 64, v232\n"
    "v_lshrrev_b32 v244, 2, v242\n"
    "v_lshl_add_u32 v245, v241, 3, v244\n"
    "v_and_b32 v246, 3, v240\n"
    "v_lshlrev_b32 v246, 3, v246\n"
    "s_lshl_b32 s71, s75, 8\n"
    "s_add_u32 s71, s71, 106496\n"
    "v_lshl_add_u32 v254, v242, 2, s71\n"
    "s_mov_b32 s83, 4294860800\n"
    "s_mov_b32 s81, 0\n"
    "s_mov_b32 s80, 0\n"
    "s_mov_b32 s84, 0xbfb8aa3b\n"
    "s_mov_b32 s85, 0xbfb8aa3b\n"
    "v_mov_b64 v[0:1], 0\n"
    "v_mov_b64 v[2:3], 0\n"
    "v_mov_b64 v[4:5], 0\n"
    "v_mov_b64 v[6:7], 0\n"
    "v_mov_b64 v[8:9], 0\n"
    "v_mov_b64 v[10:11], 0\n"
    "v_mov_b64 v[12:13], 0\n"
    "v_mov_b64 v[14:15], 0\n"
    "v_mov_b64 v[16:17], 0\n"
    "v_mov_b64 v[18:19], 0\n"
    "v_mov_b64 v[20:21], 0\n"
    "v_mov_b64 v[22:23], 0\n"
    "v_mov_b64 v[24:25], 0\n"
    "v_mov_b64 v[26:27], 0\n"
    "v_mov_b64 v[28:29], 0\n"
    "v_mov_b64 v[30:31], 0\n"
    "v_mov_b64 v[32:33], 0\n"
    "v_mov_b64 v[34:35], 0\n"
    "v_mov_b64 v[36:37], 0\n"
    "v_mov_b64 v[38:39], 0\n"
    "v_mov_b64 v[40:41], 0\n"
    "v_mov_b64 v[42:43], 0\n"
    "v_mov_b64 v[44:45], 0\n"
    "v_mov_b64 v[46:47], 0\n"
    "v_mov_b64 v[48:49], 0\n"
    "v_mov_b64 v[50:51], 0\n"
    "v_mov_b64 v[52:53], 0\n"
    "v_mov_b64 v[54:55], 0\n"
    "v_mov_b64 v[56:57], 0\n"
    "v_mov_b64 v[58:59], 0\n"
    "v_mov_b64 v[60:61], 0\n"
    "v_mov_b64 v[62:63], 0\n"
    "v_mov_b64 v[64:65], 0\n"
    "v_mov_b64 v[66:67], 0\n"
    "v_mov_b64 v[68:69], 0\n"
    "v_mov_b64 v[70:71], 0\n"
    "v_mov_b64 v[72:73], 0\n"
    "v_mov_b64 v[74:75], 0\n"
    "v_mov_b64 v[76:77], 0\n"
    "v_mov_b64 v[78:79], 0\n"
    "v_mov_b64 v[80:81], 0\n"
    "v_mov_b64 v[82:83], 0\n"
    "v_mov_b64 v[84:85], 0\n"
    "v_mov_b64 v[86:87], 0\n"
    "v_mov_b64 v[88:89], 0\n"
    "v_mov_b64 v[90:91], 0\n"
    "v_mov_b64 v[92:93], 0\n"
    "v_mov_b64 v[94:95], 0\n"
    "v_mov_b64 v[96:97], 0\n"
    "v_mov_b64 v[98:99], 0\n"
    "v_mov_b64 v[100:101], 0\n"
    "v_mov_b64 v[102:103], 0\n"
    "v_mov_b64 v[104:105], 0\n"
    "v_mov_b64 v[106:107], 0\n"
    "v_mov_b64 v[108:109], 0\n"
    "v_mov_b64 v[110:111], 0\n"
    "v_mov_b64 v[112:113], 0\n"
    "v_mov_b64 v[114:115], 0\n"
    "v_mov_b64 v[116:117], 0\n"
    "v_mov_b64 v[118:119], 0\n"
    "v_mov_b64 v[120:121], 0\n"
    "v_mov_b64 v[122:123], 0\n"
    "v_mov_b64 v[124:125], 0\n"
    "v_mov_b64 v[126:127], 0\n"
    "v_mov_b64 v[128:129], 0\n"
    "v_mov_b64 v[130:131], 0\n"
    "v_mov_b64 v[132:133], 0\n"
    "v_mov_b64 v[134:135], 0\n"
    "v_mov_b64 v[136:137], 0\n"
    "v_mov_b64 v[138:139], 0\n"
    "v_mov_b64 v[140:141], 0\n"
    "v_mov_b64 v[142:143], 0\n"
    "s_xor_b32 s77, s70, s35\n"
    "s_and_b32 s77, s77, 3\n"
    "s_lshl_b32 s91, s74, 12\n"
    "s_waitcnt lgkmcnt(0)\n"
    "s_mov_b32 s54, 8388608\n"
    "s_mov_b32 s55, 0x20000\n"
    "s_cmp_ge_u32 s34, 2\n"
    "s_cselect_b64 s[52:53], s[12:13], s[10:11]\n"
    "s_and_b32 s53, s53, 0xffff\n"
    "s_mov_b32 s58, 1835008\n"
    "s_mov_b32 s59, 0x20000\n"
    "s_mov_b32 s56, s22\n"
    "s_and_b32 s57, s23, 0xffff\n"
    "s_mov_b32 s62, 25165824\n"
    "s_mov_b32 s63, 0x20000\n"
    "s_mov_b32 s60, s28\n"
    "s_and_b32 s61, s29, 0xffff\n"
    "s_mov_b32 s66, 2097152\n"
    "s_mov_b32 s67, 0x20000\n"
    "s_mov_b32 s64, s26\n"
    "s_and_b32 s65, s27, 0xffff\n"
    "s_mul_i32 s71, s76, 2048\n"
    "s_add_u32 s71, s71, 49152\n"
    "v_add_u32 v234, s71, v243\n"
    "v_xor_b32 v235, 64, v234\n"
    "v_and_b32 v236, 1, v241\n"
    "v_xor_b32 v236, s76, v236\n"
    "v_lshl_add_u32 v236, v236, 2, v244\n"
    "v_lshl_add_u32 v236, v236, 5, v246\n"
    "v_lshl_add_u32 v236, v245, 8, v236\n"
    "v_add_u32 v236, 16384, v236\n"
    "v_xor_b32 v237, 32, v236\n"
    "v_xor_b32 v238, 64, v236\n"
    "v_xor_b32 v239, 96, v236\n"
    "s_lshr_b32 s71, s34, 1\n"
    "s_lshl4_add_u32 s71, s71, s70\n"
    "s_lshl1_add_u32 s71, s71, s75\n"
    "s_and_b32 s72, s34, 1\n"
    "s_lshl1_add_u32 s71, s71, s72\n"
    "s_lshl3_add_u32 s71, s71, s35\n"
    "s_mul_i32 s72, s76, 1\n"
    "s_lshl1_add_u32 s71, s71, s72\n"
    "s_lshl_b32 s93, s71, 11\n"
    "s_add_u32 s94, s93, 4096\n"
    "s_add_u32 s95, s93, 8192\n"
    "s_lshl2_add_u32 s71, s70, s34\n"
    "s_mul_i32 s71, s71, 7\n"
    "s_lshl1_add_u32 s71, s35, s71\n"
    "s_add_u32 s71, s71, s76\n"
    "s_lshl1_add_u32 s71, s71, s75\n"
    "s_lshl_b32 s71, s71, 13\n"
    "s_add_u32 s96, s71, 4194304\n"
    "s_add_u32 s97, s71, 4198400\n"
    "v_and_b32 v243, 7, v240\n"
    "v_bfe_u32 v244, v240, 3, 1\n"
    "v_lshl_add_u32 v243, v243, 3, v244\n"
    "s_and_b32 s71, s34, 1\n"
    "s_lshl_b32 s71, s71, 4\n"
    "s_and_b32 s72, s34, 2\n"
    "s_add_u32 s71, s71, s72\n"
    "s_lshr_b32 s72, s70, 3\n"
    "s_add_u32 s71, s71, s72\n"
    "s_lshl2_add_u32 s71, s71, s35\n"
    "s_and_b32 s72, s70, 7\n"
    "s_lshl4_add_u32 s71, s71, s72\n"
    "s_lshl3_add_u32 s71, s71, 20971520\n"
    "v_lshl_add_u32 v243, v243, 11, s71\n"
    "v_xor_b32 v243, 12288, v243\n"
    "s_waitcnt vmcnt(0)\n"
    "s_mov_b64 exec, 0\n"
    "s_cmp_eq_u32 s74, 0\n"
    "s_cbranch_scc0 .LW30_%=\n"
    "s_call_b64 s[18:19], .LPAD0_%=\n"
    ".LW30_%=:\n"
    "s_cmp_eq_u32 s74, 1\n"
    "s_cbranch_scc0 .LW31_%=\n"
    "s_call_b64 s[18:19], .LPAD3_%=\n"
    ".LW31_%=:\n"
    "s_cmp_eq_u32 s74, 2\n"
    "s_cbranch_scc0 .LW32_%=\n"
    "s_nop 0\n"
    ".LW32_%=:\n"
    "s_cmp_eq_u32 s74, 3\n"
    "s_cbranch_scc0 .LW33_%=\n"
    "s_nop 0\n"
    ".LW33_%=:\n"
    "s_mov_b64 exec, -1\n"
    "s_barrier\n"
    "ds_read_b128 v[144:147], v232 offset:0\n"
    "ds_read_b128 v[148:151], v233 offset:0\n"
    "ds_read_b128 v[208:211], v234 offset:0\n"
    "ds_read_b128 v[212:215], v235 offset:0\n"
    "ds_read_b128 v[152:155], v232 offset:2048\n"
    "ds_read_b128 v[156:159], v233 offset:2048\n"
    "ds_read_b128 v[160:163], v232 offset:4096\n"
    "ds_read_b128 v[164:167], v233 offset:4096\n"
    "ds_read_b128 v[168:171], v232 offset:6144\n"
    "ds_read_b128 v[172:175], v233 offset:6144\n"
    "ds_read_b64_tr_b16 v[216:217], v236 offset:0\n"
    "ds_read_b64_tr_b16 v[218:219], v236 offset:1024\n"
    "ds_read_b64_tr_b16 v[220:221], v236 offset:8192\n"
    "ds_read_b64_tr_b16 v[222:223], v236 offset:9216\n"
    "s_nop 0\n"
    ".LAL12X_%=:\n"
    "s_waitcnt lgkmcnt(10)\n"
    "v_mfma_f32_16x16x32_bf16 v[0:3], v[144:147], v[208:211], v[0:3]\n"
    "ds_read_b64_tr_b16 v[224:225], v236 offset:16384\n"
    "v_mfma_f32_16x16x32_bf16 v[0:3], v[148:151], v[212:215], v[0:3]\n"
    "ds_read_b64_tr_b16 v[226:227], v236 offset:17408\n"
    "s_waitcnt lgkmcnt(10)\n"
    "v_mfma_f32_16x16x32_bf16 v[4:7], v[152:155], v[208:211], v[4:7]\n"
    "ds_read_b64_tr_b16 v[228:229], v236 offset:24576\n"
    "v_mfma_f32_16x16x32_bf16 v[4:7], v[156:159], v[212:215], v[4:7]\n"
    "ds_read_b64_tr_b16 v[230:231], v236 offset:25600\n"
    "s_waitcnt lgkmcnt(10)\n"
    "v_mfma_f32_16x16x32_bf16 v[8:11], v[160:163], v[208:211], v[8:11]\n"
    "s_add_u32 s81, s81, 1\n"
    "s_cmp_eq_u32 s81, 3\n"
    "v_mfma_f32_16x16x32_bf16 v[8:11], v[164:167], v[212:215], v[8:11]\n"
    "s_cselect_b32 s82, s83, 53248\n"
    "s_waitcnt lgkmcnt(8)\n"
    "v_mfma_f32_16x16x32_bf16 v[12:15], v[168:171], v[208:211], v[12:15]\n"
    "s_cselect_b32 s81, 0, s81\n"
    "v_mfma_f32_16x16x32_bf16 v[12:15], v[172:175], v[212:215], v[12:15]\n"
    "s_waitcnt lgkmcnt(4)\n"
    "v_mfma_f32_16x16x32_bf16 v[28:31], v[216:219], v[168:171], v[28:31]\n"
    "ds_read_b64_tr_b16 v[208:209], v237 offset:0\n"
    "v_mfma_f32_16x16x32_bf16 v[28:31], v[220:223], v[172:175], v[28:31]\n"
    "ds_read_b64_tr_b16 v[210:211], v237 offset:1024\n"
    "v_mfma_f32_16x16x32_bf16 v[24:27], v[216:219], v[160:163], v[24:27]\n"
    "ds_read_b64_tr_b16 v[212:213], v237 offset:8192\n"
    "v_mfma_f32_16x16x32_bf16 v[24:27], v[220:223], v[164:167], v[24:27]\n"
    "ds_read_b64_tr_b16 v[214:215], v237 offset:9216\n"
    "v_mfma_f32_16x16x32_bf16 v[20:23], v[216:219], v[152:155], v[20:23]\n"
    "v_add_u32_e64 v232, s82, v232\n"
    "v_mfma_f32_16x16x32_bf16 v[20:23], v[220:223], v[156:159], v[20:23]\n"
    "v_add_u32_e64 v233, s82, v233\n"
    "v_mfma_f32_16x16x32_bf16 v[16:19], v[216:219], v[144:147], v[16:19]\n"
    "v_add_u32_e64 v236, s82, v236\n"
    "v_mfma_f32_16x16x32_bf16 v[16:19], v[220:223], v[148:151], v[16:19]\n"
    "v_add_u32_e64 v234, s82, v234\n"
    "s_waitcnt lgkmcnt(4)\n"
    "v_mfma_f32_16x16x32_bf16 v[32:35], v[224:227], v[144:147], v[32:35]\n"
    "ds_read_b64_tr_b16 v[216:217], v237 offset:16384\n"
    "v_mfma_f32_16x16x32_bf16 v[32:35], v[228:231], v[148:151], v[32:35]\n"
    "ds_read_b64_tr_b16 v[218:219], v237 offset:17408\n"
    "v_mfma_f32_16x16x32_bf16 v[36:39], v[224:227], v[152:155], v[36:39]\n"
    "ds_read_b64_tr_b16 v[220:221], v237 offset:24576\n"
    "v_mfma_f32_16x16x32_bf16 v[36:39], v[228:231], v[156:159], v[36:39]\n"
    "ds_read_b64_tr_b16 v[222:223], v237 offset:25600\n"
    "v_mfma_f32_16x16x32_bf16 v[40:43], v[224:227], v[160:163], v[40:43]\n"
    "v_add_u32_e64 v237, s82, v237\n"
    "v_mfma_f32_16x16x32_bf16 v[40:43], v[228:231], v[164:167], v[40:43]\n"
    "v_add_u32_e64 v235, s82, v235\n"
    "v_mfma_f32_16x16x32_bf16 v[44:47], v[224:227], v[168:171], v[44:47]\n"
    "v_mfma_f32_16x16x32_bf16 v[44:47], v[228:231], v[172:175], v[44:47]\n"
    "s_waitcnt lgkmcnt(4)\n"
    "v_mfma_f32_16x16x32_bf16 v[60:63], v[208:211], v[168:171], v[60:63]\n"
    "ds_read_b64_tr_b16 v[224:225], v238 offset:0\n"
    "v_mfma_f32_16x16x32_bf16 v[60:63], v[212:215], v[172:175], v[60:63]\n"
    "ds_read_b64_tr_b16 v[226:227], v238 offset:1024\n"
    "v_mfma_f32_16x16x32_bf16 v[56:59], v[208:211], v[160:163], v[56:59]\n"
    "ds_read_b64_tr_b16 v[228:229], v238 offset:8192\n"
    "v_mfma_f32_16x16x32_bf16 v[56:59], v[212:215], v[164:167], v[56:59]\n"
    "ds_read_b64_tr_b16 v[230:231], v238 offset:9216\n"
    "v_mfma_f32_16x16x32_bf16 v[52:55], v[208:211], v[152:155], v[52:55]\n"
    "v_mfma_f32_16x16x32_bf16 v[52:55], v[212:215], v[156:159], v[52:55]\n"
    "v_mfma_f32_16x16x32_bf16 v[48:51], v[208:211], v[144:147], v[48:51]\n"
    "v_mfma_f32_16x16x32_bf16 v[48:51], v[212:215], v[148:151], v[48:51]\n"
    "s_waitcnt lgkmcnt(4)\n"
    "v_mfma_f32_16x16x32_bf16 v[64:67], v[216:219], v[144:147], v[64:67]\n"
    "ds_read_b64_tr_b16 v[208:209], v238 offset:16384\n"
    "v_mfma_f32_16x16x32_bf16 v[64:67], v[220:223], v[148:151], v[64:67]\n"
    "ds_read_b64_tr_b16 v[210:211], v238 offset:17408\n"
    "v_mfma_f32_16x16x32_bf16 v[68:71], v[216:219], v[152:155], v[68:71]\n"
    "ds_read_b64_tr_b16 v[212:213], v238 offset:24576\n"
    "v_mfma_f32_16x16x32_bf16 v[68:71], v[220:223], v[156:159], v[68:71]\n"
    "ds_read_b64_tr_b16 v[214:215], v238 offset:25600\n"
    "v_mfma_f32_16x16x32_bf16 v[72:75], v[216:219], v[160:163], v[72:75]\n"
    "v_add_u32_e64 v238, s82, v238\n"
    "v_mfma_f32_16x16x32_bf16 v[72:75], v[220:223], v[164:167], v[72:75]\n"
    "v_mfma_f32_16x16x32_bf16 v[76:79], v[216:219], v[168:171], v[76:79]\n"
    "v_mfma_f32_16x16x32_bf16 v[76:79], v[220:223], v[172:175], v[76:79]\n"
    "s_waitcnt lgkmcnt(4)\n"
    "v_mfma_f32_16x16x32_bf16 v[92:95], v[224:227], v[168:171], v[92:95]\n"
    "ds_read_b64_tr_b16 v[216:217], v239 offset:0\n"
    "v_mfma_f32_16x16x32_bf16 v[92:95], v[228:231], v[172:175], v[92:95]\n"
    "ds_read_b64_tr_b16 v[218:219], v239 offset:1024\n"
    "v_mfma_f32_16x16x32_bf16 v[88:91], v[224:227], v[160:163], v[88:91]\n"
    "ds_read_b64_tr_b16 v[220:221], v239 offset:8192\n"
    "v_mfma_f32_16x16x32_bf16 v[88:91], v[228:231], v[164:167], v[88:91]\n"
    "ds_read_b64_tr_b16 v[222:223], v239 offset:9216\n"
    "v_mfma_f32_16x16x32_bf16 v[84:87], v[224:227], v[152:155], v[84:87]\n"
    "v_mfma_f32_16x16x32_bf16 v[84:87], v[228:231], v[156:159], v[84:87]\n"
    "v_mfma_f32_16x16x32_bf16 v[80:83], v[224:227], v[144:147], v[80:83]\n"
    "v_mfma_f32_16x16x32_bf16 v[80:83], v[228:231], v[148:151], v[80:83]\n"
    "s_waitcnt lgkmcnt(4)\n"
    "v_mfma_f32_16x16x32_bf16 v[96:99], v[208:211], v[144:147], v[96:99]\n"
    "ds_read_b64_tr_b16 v[224:225], v239 offset:16384\n"
    "v_mfma_f32_16x16x32_bf16 v[96:99], v[212:215], v[148:151], v[96:99]\n"
    "ds_read_b64_tr_b16 v[226:227], v239 offset:17408\n"
    "v_mfma_f32_16x16x32_bf16 v[100:103], v[208:211], v[152:155], v[100:103]\n"
    "ds_read_b64_tr_b16 v[228:229], v239 offset:24576\n"
    "v_mfma_f32_16x16x32_bf16 v[100:103], v[212:215], v[156:159], v[100:103]\n"
    "ds_read_b64_tr_b16 v[230:231], v239 offset:25600\n"
    "v_mfma_f32_16x16x32_bf16 v[104:107], v[208:211], v[160:163], v[104:107]\n"
    "v_add_u32_e64 v239, s82, v239\n"
    "v_mfma_f32_16x16x32_bf16 v[104:107], v[212:215], v[164:167], v[104:107]\n"
    "v_mfma_f32_16x16x32_bf16 v[108:111], v[208:211], v[168:171], v[108:111]\n"
    "v_mfma_f32_16x16x32_bf16 v[108:111], v[212:215], v[172:175], v[108:111]\n"
    "s_waitcnt lgkmcnt(4)\n"
    "v_mfma_f32_16x16x32_bf16 v[124:127], v[216:219], v[168:171], v[124:127]\n"
    "v_mfma_f32_16x16x32_bf16 v[124:127], v[220:223], v[172:175], v[124:127]\n"
    "s_waitcnt lgkmcnt(0)\n"
    "s_barrier\n"
    "v_mfma_f32_16x16x32_bf16 v[120:123], v[216:219], v[160:163], v[120:123]\n"
    "ds_read_b128 v[176:179], v232 offset:0\n"
    "v_mfma_f32_16x16x32_bf16 v[120:123], v[220:223], v[164:167], v[120:123]\n"
    "ds_read_b128 v[180:183], v233 offset:0\n"
    "v_mfma_f32_16x16x32_bf16 v[116:119], v[216:219], v[152:155], v[116:119]\n"
    "ds_read_b128 v[208:211], v234 offset:0\n"
    "v_mfma_f32_16x16x32_bf16 v[116:119], v[220:223], v[156:159], v[116:119]\n"
    "ds_read_b128 v[212:215], v235 offset:0\n"
    "v_mfma_f32_16x16x32_bf16 v[112:115], v[216:219], v[144:147], v[112:115]\n"
    "ds_read_b128 v[184:187], v232 offset:2048\n"
    "v_mfma_f32_16x16x32_bf16 v[112:115], v[220:223], v[148:151], v[112:115]\n"
    "ds_read_b128 v[188:191], v233 offset:2048\n"
    "v_mfma_f32_16x16x32_bf16 v[128:131], v[224:227], v[144:147], v[128:131]\n"
    "ds_read_b128 v[192:195], v232 offset:4096\n"
    "v_mfma_f32_16x16x32_bf16 v[128:131], v[228:231], v[148:151], v[128:131]\n"
    "ds_read_b128 v[196:199], v233 offset:4096\n"
    "v_mfma_f32_16x16x32_bf16 v[132:135], v[224:227], v[152:155], v[132:135]\n"
    "ds_read_b128 v[200:203], v232 offset:6144\n"
    "v_mfma_f32_16x16x32_bf16 v[132:135], v[228:231], v[156:159], v[132:135]\n"
    "ds_read_b128 v[204:207], v233 offset:6144\n"
    "v_mfma_f32_16x16x32_bf16 v[136:139], v[224:227], v[160:163], v[136:139]\n"
    "ds_read_b64_tr_b16 v[216:217], v236 offset:0\n"
    "v_mfma_f32_16x16x32_bf16 v[136:139], v[228:231], v[164:167], v[136:139]\n"
    "ds_read_b64_tr_b16 v[218:219], v236 offset:1024\n"
    "v_mfma_f32_16x16x32_bf16 v[140:143], v[224:227], v[168:171], v[140:143]\n"
    "ds_read_b64_tr_b16 v[220:221], v236 offset:8192\n"
    "v_mfma_f32_16x16x32_bf16 v[140:143], v[228:231], v[172:175], v[140:143]\n"
    "ds_read_b64_tr_b16 v[222:223], v236 offset:9216\n"
    "s_waitcnt lgkmcnt(10)\n"
    "v_mfma_f32_16x16x32_bf16 v[0:3], v[176:179], v[208:211], v[0:3]\n"
    "ds_read_b64_tr_b16 v[224:225], v236 offset:16384\n"
    "v_mfma_f32_16x16x32_bf16 v[0:3], v[180:183], v[212:215], v[0:3]\n"
    "ds_read_b64_tr_b16 v[226:227], v236 offset:17408\n"
    "s_waitcnt lgkmcnt(10)\n"
    "v_mfma_f32_16x16x32_bf16 v[4:7], v[184:187], v[208:211], v[4:7]\n"
    "ds_read_b64_tr_b16 v[228:229], v236 offset:24576\n"
    "v_mfma_f32_16x16x32_bf16 v[4:7], v[188:191], v[212:215], v[4:7]\n"
    "ds_read_b64_tr_b16 v[230:231], v236 offset:25600\n"
    "s_waitcnt lgkmcnt(10)\n"
    "v_mfma_f32_16x16x32_bf16 v[8:11], v[192:195], v[208:211], v[8:11]\n"
    "s_add_u32 s81, s81, 1\n"
    "s_cmp_eq_u32 s81, 3\n"
    "v_mfma_f32_16x16x32_bf16 v[8:11], v[196:199], v[212:215], v[8:11]\n"
    "s_cselect_b32 s82, s83, 53248\n"
    "s_waitcnt lgkmcnt(8)\n"
    "v_mfma_f32_16x16x32_bf16 v[12:15], v[200:203], v[208:211], v[12:15]\n"
    "s_cselect_b32 s81, 0, s81\n"
    "v_mfma_f32_16x16x32_bf16 v[12:15], v[204:207], v[212:215], v[12:15]\n"
    "s_add_u32 s80, s80, 1\n"
    "s_cmp_lt_u32 s80, 3\n"
    "s_waitcnt lgkmcnt(4)\n"
    "v_mfma_f32_16x16x32_bf16 v[28:31], v[216:219], v[200:203], v[28:31]\n"
    "ds_read_b64_tr_b16 v[208:209], v237 offset:0\n"
    "v_mfma_f32_16x16x32_bf16 v[28:31], v[220:223], v[204:207], v[28:31]\n"
    "ds_read_b64_tr_b16 v[210:211], v237 offset:1024\n"
    "v_mfma_f32_16x16x32_bf16 v[24:27], v[216:219], v[192:195], v[24:27]\n"
    "ds_read_b64_tr_b16 v[212:213], v237 offset:8192\n"
    "v_mfma_f32_16x16x32_bf16 v[24:27], v[220:223], v[196:199], v[24:27]\n"
    "ds_read_b64_tr_b16 v[214:215], v237 offset:9216\n"
    "v_mfma_f32_16x16x32_bf16 v[20:23], v[216:219], v[184:187], v[20:23]\n"
    "v_add_u32_e64 v232, s82, v232\n"
    "v_mfma_f32_16x16x32_bf16 v[20:23], v[220:223], v[188:191], v[20:23]\n"
    "v_add_u32_e64 v233, s82, v233\n"
    "v_mfma_f32_16x16x32_bf16 v[16:19], v[216:219], v[176:179], v[16:19]\n"
    "v_add_u32_e64 v236, s82, v236\n"
    "v_mfma_f32_16x16x32_bf16 v[16:19], v[220:223], v[180:183], v[16:19]\n"
    "v_add_u32_e64 v234, s82, v234\n"
    "s_waitcnt lgkmcnt(4)\n"
    "v_mfma_f32_16x16x32_bf16 v[32:35], v[224:227], v[176:179], v[32:35]\n"
    "ds_read_b64_tr_b16 v[216:217], v237 offset:16384\n"
    "v_mfma_f32_16x16x32_bf16 v[32:35], v[228:231], v[180:183], v[32:35]\n"
    "ds_read_b64_tr_b16 v[218:219], v237 offset:17408\n"
    "v_mfma_f32_16x16x32_bf16 v[36:39], v[224:227], v[184:187], v[36:39]\n"
    "ds_read_b64_tr_b16 v[220:221], v237 offset:24576\n"
    "v_mfma_f32_16x16x32_bf16 v[36:39], v[228:231], v[188:191], v[36:39]\n"
    "ds_read_b64_tr_b16 v[222:223], v237 offset:25600\n"
    "v_mfma_f32_16x16x32_bf16 v[40:43], v[224:227], v[192:195], v[40:43]\n"
    "v_add_u32_e64 v237, s82, v237\n"
    "v_mfma_f32_16x16x32_bf16 v[40:43], v[228:231], v[196:199], v[40:43]\n"
    "v_add_u32_e64 v235, s82, v235\n"
    "v_mfma_f32_16x16x32_bf16 v[44:47], v[224:227], v[200:203], v[44:47]\n"
    "v_mfma_f32_16x16x32_bf16 v[44:47], v[228:231], v[204:207], v[44:47]\n"
    "s_waitcnt lgkmcnt(4)\n"
    "v_mfma_f32_16x16x32_bf16 v[60:63], v[208:211], v[200:203], v[60:63]\n"
    "ds_read_b64_tr_b16 v[224:225], v238 offset:0\n"
    "v_mfma_f32_16x16x32_bf16 v[60:63], v[212:215], v[204:207], v[60:63]\n"
    "ds_read_b64_tr_b16 v[226:227], v238 offset:1024\n"
    "v_mfma_f32_16x16x32_bf16 v[56:59], v[208:211], v[192:195], v[56:59]\n"
    "ds_read_b64_tr_b16 v[228:229], v238 offset:8192\n"
    "v_mfma_f32_16x16x32_bf16 v[56:59], v[212:215], v[196:199], v[56:59]\n"
    "ds_read_b64_tr_b16 v[230:231], v238 offset:9216\n"
    "v_mfma_f32_16x16x32_bf16 v[52:55], v[208:211], v[184:187], v[52:55]\n"
    "v_mfma_f32_16x16x32_bf16 v[52:55], v[212:215], v[188:191], v[52:55]\n"
    "v_mfma_f32_16x16x32_bf16 v[48:51], v[208:211], v[176:179], v[48:51]\n"
    "v_mfma_f32_16x16x32_bf16 v[48:51], v[212:215], v[180:183], v[48:51]\n"
    "s_waitcnt lgkmcnt(4)\n"
    "v_mfma_f32_16x16x32_bf16 v[64:67], v[216:219], v[176:179], v[64:67]\n"
    "ds_read_b64_tr_b16 v[208:209], v238 offset:16384\n"
    "v_mfma_f32_16x16x32_bf16 v[64:67], v[220:223], v[180:183], v[64:67]\n"
    "ds_read_b64_tr_b16 v[210:211], v238 offset:17408\n"
    "v_mfma_f32_16x16x32_bf16 v[68:71], v[216:219], v[184:187], v[68:71]\n"
    "ds_read_b64_tr_b16 v[212:213], v238 offset:24576\n"
    "v_mfma_f32_16x16x32_bf16 v[68:71], v[220:223], v[188:191], v[68:71]\n"
    "ds_read_b64_tr_b16 v[214:215], v238 offset:25600\n"
    "v_mfma_f32_16x16x32_bf16 v[72:75], v[216:219], v[192:195], v[72:75]\n"
    "v_add_u32_e64 v238, s82, v238\n"
    "v_mfma_f32_16x16x32_bf16 v[72:75], v[220:223], v[196:199], v[72:75]\n"
    "v_mfma_f32_16x16x32_bf16 v[76:79], v[216:219], v[200:203], v[76:79]\n"
    "v_mfma_f32_16x16x32_bf16 v[76:79], v[220:223], v[204:207], v[76:79]\n"
    "s_waitcnt lgkmcnt(4)\n"
    "v_mfma_f32_16x16x32_bf16 v[92:95], v[224:227], v[200:203], v[92:95]\n"
    "ds_read_b64_tr_b16 v[216:217], v239 offset:0\n"
    "v_mfma_f32_16x16x32_bf16 v[92:95], v[228:231], v[204:207], v[92:95]\n"
    "ds_read_b64_tr_b16 v[218:219], v239 offset:1024\n"
    "v_mfma_f32_16x16x32_bf16 v[88:91], v[224:227], v[192:195], v[88:91]\n"
    "ds_read_b64_tr_b16 v[220:221], v239 offset:8192\n"
    "v_mfma_f32_16x16x32_bf16 v[88:91], v[228:231], v[196:199], v[88:91]\n"
    "ds_read_b64_tr_b16 v[222:223], v239 offset:9216\n"
    "v_mfma_f32_16x16x32_bf16 v[84:87], v[224:227], v[184:187], v[84:87]\n"
    "v_mfma_f32_16x16x32_bf16 v[84:87], v[228:231], v[188:191], v[84:87]\n"
    "v_mfma_f32_16x16x32_bf16 v[80:83], v[224:227], v[176:179], v[80:83]\n"
    "v_mfma_f32_16x16x32_bf16 v[80:83], v[228:231], v[180:183], v[80:83]\n"
    "s_waitcnt lgkmcnt(4)\n"
    "v_mfma_f32_16x16x32_bf16 v[96:99], v[208:211], v[176:179], v[96:99]\n"
    "ds_read_b64_tr_b16 v[224:225], v239 offset:16384\n"
    "v_mfma_f32_16x16x32_bf16 v[96:99], v[212:215], v[180:183], v[96:99]\n"
    "ds_read_b64_tr_b16 v[226:227], v239 offset:17408\n"
    "v_mfma_f32_16x16x32_bf16 v[100:103], v[208:211], v[184:187], v[100:103]\n"
    "ds_read_b64_tr_b16 v[228:229], v239 offset:24576\n"
    "v_mfma_f32_16x16x32_bf16 v[100:103], v[212:215], v[188:191], v[100:103]\n"
    "ds_read_b64_tr_b16 v[230:231], v239 offset:25600\n"
    "v_mfma_f32_16x16x32_bf16 v[104:107], v[208:211], v[192:195], v[104:107]\n"
    "v_add_u32_e64 v239, s82, v239\n"
    "v_mfma_f32_16x16x32_bf16 v[104:107], v[212:215], v[196:199], v[104:107]\n"
    "v_mfma_f32_16x16x32_bf16 v[108:111], v[208:211], v[200:203], v[108:111]\n"
    "v_mfma_f32_16x16x32_bf16 v[108:111], v[212:215], v[204:207], v[108:111]\n"
    "s_waitcnt lgkmcnt(4)\n"
    "v_mfma_f32_16x16x32_bf16 v[124:127], v[216:219], v[200:203], v[124:127]\n"
    "v_mfma_f32_16x16x32_bf16 v[124:127], v[220:223], v[204:207], v[124:127]\n"
    "s_waitcnt lgkmcnt(0)\n"
    "s_barrier\n"
    "v_mfma_f32_16x16x32_bf16 v[120:123], v[216:219], v[192:195], v[120:123]\n"
    "ds_read_b128 v[144:147], v232 offset:0\n"
    "v_mfma_f32_16x16x32_bf16 v[120:123], v[220:223], v[196:199], v[120:123]\n"
    "ds_read_b128 v[148:151], v233 offset:0\n"
    "v_mfma_f32_16x16x32_bf16 v[116:119], v[216:219], v[184:187], v[116:119]\n"
    "ds_read_b128 v[208:211], v234 offset:0\n"
    "v_mfma_f32_16x16x32_bf16 v[116:119], v[220:223], v[188:191], v[116:119]\n"
    "ds_read_b128 v[212:215], v235 offset:0\n"
    "v_mfma_f32_16x16x32_bf16 v[112:115], v[216:219], v[176:179], v[112:115]\n"
    "ds_read_b128 v[152:155], v232 offset:2048\n"
    "v_mfma_f32_16x16x32_bf16 v[112:115], v[220:223], v[180:183], v[112:115]\n"
    "ds_read_b128 v[156:159], v233 offset:2048\n"
    "v_mfma_f32_16x16x32_bf16 v[128:131], v[224:227], v[176:179], v[128:131]\n"
    "ds_read_b128 v[160:163], v232 offset:4096\n"
    "v_mfma_f32_16x16x32_bf16 v[128:131], v[228:231], v[180:183], v[128:131]\n"
    "ds_read_b128 v[164:167], v233 offset:4096\n"
    "v_mfma_f32_16x16x32_bf16 v[132:135], v[224:227], v[184:187], v[132:135]\n"
    "ds_read_b128 v[168:171], v232 offset:6144\n"
    "v_mfma_f32_16x16x32_bf16 v[132:135], v[228:231], v[188:191], v[132:135]\n"
    "ds_read_b128 v[172:175], v233 offset:6144\n"
    "v_mfma_f32_16x16x32_bf16 v[136:139], v[224:227], v[192:195], v[136:139]\n"
    "ds_read_b64_tr_b16 v[216:217], v236 offset:0\n"
    "v_mfma_f32_16x16x32_bf16 v[136:139], v[228:231], v[196:199], v[136:139]\n"
    "ds_read_b64_tr_b16 v[218:219], v236 offset:1024\n"
    "v_mfma_f32_16x16x32_bf16 v[140:143], v[224:227], v[200:203], v[140:143]\n"
    "ds_read_b64_tr_b16 v[220:221], v236 offset:8192\n"
    "v_mfma_f32_16x16x32_bf16 v[140:143], v[228:231], v[204:207], v[140:143]\n"
    "ds_read_b64_tr_b16 v[222:223], v236 offset:9216\n"
    "s_cbranch_scc1 .LAL12X_%=\n"
    "s_waitcnt lgkmcnt(10)\n"
    "v_mfma_f32_16x16x32_bf16 v[0:3], v[144:147], v[208:211], v[0:3]\n"
    "ds_read_b128 v[224:227], v234 offset:53248\n"
    "v_mfma_f32_16x16x32_bf16 v[0:3], v[148:151], v[212:215], v[0:3]\n"
    "ds_read_b128 v[228:231], v235 offset:53248\n"
    "s_waitcnt lgkmcnt(10)\n"
    "v_mfma_f32_16x16x32_bf16 v[4:7], v[152:155], v[208:211], v[4:7]\n"
    "ds_read_b128 v[176:179], v232 offset:53248\n"
    "v_mfma_f32_16x16x32_bf16 v[4:7], v[156:159], v[212:215], v[4:7]\n"
    "ds_read_b128 v[180:183], v233 offset:53248\n"
    "s_waitcnt lgkmcnt(10)\n"
    "v_mfma_f32_16x16x32_bf16 v[8:11], v[160:163], v[208:211], v[8:11]\n"
    "ds_read_b128 v[184:187], v232 offset:55296\n"
    "v_mfma_f32_16x16x32_bf16 v[8:11], v[164:167], v[212:215], v[8:11]\n"
    "ds_read_b128 v[188:191], v233 offset:55296\n"
    "s_waitcnt lgkmcnt(10)\n"
    "v_mfma_f32_16x16x32_bf16 v[12:15], v[168:171], v[208:211], v[12:15]\n"
    "ds_read_b128 v[192:195], v232 offset:57344\n"
    "v_mfma_f32_16x16x32_bf16 v[12:15], v[172:175], v[212:215], v[12:15]\n"
    "ds_read_b128 v[196:199], v233 offset:57344\n"
    "s_waitcnt lgkmcnt(8)\n"
    "v_mfma_f32_16x16x32_bf16 v[28:31], v[216:219], v[168:171], v[28:31]\n"
    "ds_read_b64_tr_b16 v[208:209], v236 offset:16384\n"
    "v_mfma_f32_16x16x32_bf16 v[28:31], v[220:223], v[172:175], v[28:31]\n"
    "ds_read_b64_tr_b16 v[210:211], v236 offset:17408\n"
    "v_mfma_f32_16x16x32_bf16 v[24:27], v[216:219], v[160:163], v[24:27]\n"
    "ds_read_b64_tr_b16 v[212:213], v236 offset:24576\n"
    "v_mfma_f32_16x16x32_bf16 v[24:27], v[220:223], v[164:167], v[24:27]\n"
    "ds_read_b64_tr_b16 v[214:215], v236 offset:25600\n"
    "v_mfma_f32_16x16x32_bf16 v[20:23], v[216:219], v[152:155], v[20:23]\n"
    "ds_read_b128 v[200:203], v232 offset:59392\n"
    "v_mfma_f32_16x16x32_bf16 v[20:23], v[220:223], v[156:159], v[20:23]\n"
    "ds_read_b128 v[204:207], v233 offset:59392\n"
    "v_mfma_f32_16x16x32_bf16 v[16:19], v[216:219], v[144:147], v[16:19]\n"
    "v_mfma_f32_16x16x32_bf16 v[16:19], v[220:223], v[148:151], v[16:19]\n"
    "s_waitcnt lgkmcnt(10)\n"
    "v_mfma_f32_16x16x32_bf16 v[0:3], v[176:179], v[224:227], v[0:3]\n"
    "ds_read_b64_tr_b16 v[216:217], v237 offset:0\n"
    "v_mfma_f32_16x16x32_bf16 v[0:3], v[180:183], v[228:231], v[0:3]\n"
    "ds_read_b64_tr_b16 v[218:219], v237 offset:1024\n"
    "s_waitcnt lgkmcnt(10)\n"
    "v_mfma_f32_16x16x32_bf16 v[4:7], v[184:187], v[224:227], v[4:7]\n"
    "ds_read_b64_tr_b16 v[220:221], v237 offset:8192\n"
    "v_mfma_f32_16x16x32_bf16 v[4:7], v[188:191], v[228:231], v[4:7]\n"
    "ds_read_b64_tr_b16 v[222:223], v237 offset:9216\n"
    "s_waitcnt lgkmcnt(10)\n"
    "v_mfma_f32_16x16x32_bf16 v[8:11], v[192:195], v[224:227], v[8:11]\n"
    "v_mfma_f32_16x16x32_bf16 v[8:11], v[196:199], v[228:231], v[8:11]\n"
    "s_waitcnt lgkmcnt(4)\n"
    "v_mfma_f32_16x16x32_bf16 v[12:15], v[200:203], v[224:227], v[12:15]\n"
    "v_mfma_f32_16x16x32_bf16 v[12:15], v[204:207], v[228:231], v[12:15]\n"
    "s_nop 0\n"
    "s_barrier\n"
    "v_mfma_f32_16x16x32_bf16 v[44:47], v[208:211], v[168:171], v[44:47]\n"
    "ds_read_b64_tr_b16 v[224:225], v237 offset:16384\n"
    "v_mfma_f32_16x16x32_bf16 v[44:47], v[212:215], v[172:175], v[44:47]\n"
    "ds_read_b64_tr_b16 v[226:227], v237 offset:17408\n"
    "v_mfma_f32_16x16x32_bf16 v[40:43], v[208:211], v[160:163], v[40:43]\n"
    "ds_read_b64_tr_b16 v[228:229], v237 offset:24576\n"
    "v_mfma_f32_16x16x32_bf16 v[40:43], v[212:215], v[164:167], v[40:43]\n"
    "ds_read_b64_tr_b16 v[230:231], v237 offset:25600\n"
    "v_mfma_f32_16x16x32_bf16 v[36:39], v[208:211], v[152:155], v[36:39]\n"
    "ds_read_b32 v248, v254 offset:0\n"
    "v_cvt_pk_bf16_f32 v0, v0, v1\n"
    "v_mfma_f32_16x16x32_bf16 v[36:39], v[212:215], v[156:159], v[36:39]\n"
    "ds_read_b32 v249, v254 offset:64\n"
    "v_cvt_pk_bf16_f32 v1, v2, v3\n"
    "v_mfma_f32_16x16x32_bf16 v[32:35], v[208:211], v[144:147], v[32:35]\n"
    "ds_read_b32 v250, v254 offset:128\n"
    "v_cvt_pk_bf16_f32 v2, v4, v5\n"
    "v_mfma_f32_16x16x32_bf16 v[32:35], v[212:215], v[148:151], v[32:35]\n"
    "v_add_u32 v236, 53248, v236\n"
    "ds_read_b32 v251, v254 offset:192\n"
    "v_cvt_pk_bf16_f32 v3, v6, v7\n"
    "buffer_store_dwordx4 v[0:3], v247, s[60:63], s93 offen offset:0 sc1\n"
    "s_waitcnt lgkmcnt(8)\n"
    "v_mfma_f32_16x16x32_bf16 v[48:51], v[216:219], v[144:147], v[48:51]\n"
    "ds_read_b64_tr_b16 v[208:209], v236 offset:0\n"
    "v_cvt_pk_bf16_f32 v8, v8, v9\n"
    "v_mfma_f32_16x16x32_bf16 v[48:51], v[220:223], v[148:151], v[48:51]\n"
    "ds_read_b64_tr_b16 v[210:211], v236 offset:1024\n"
    "v_cvt_pk_bf16_f32 v9, v10, v11\n"
    "v_mfma_f32_16x16x32_bf16 v[52:55], v[216:219], v[152:155], v[52:55]\n"
    "ds_read_b64_tr_b16 v[212:213], v236 offset:8192\n"
    "v_cvt_pk_bf16_f32 v10, v12, v13\n"
    "v_mfma_f32_16x16x32_bf16 v[52:55], v[220:223], v[156:159], v[52:55]\n"
    "ds_read_b64_tr_b16 v[214:215], v236 offset:9216\n"
    "v_cvt_pk_bf16_f32 v11, v14, v15\n"
    "buffer_store_dwordx4 v[8:11], v247, s[60:63], s93 offen offset:1024 sc1\n"
    "v_mfma_f32_16x16x32_bf16 v[56:59], v[216:219], v[160:163], v[56:59]\n"
    "ds_read2st64_b32 v[252:253], v254 offset0:4 offset1:6\n"
    "v_mfma_f32_16x16x32_bf16 v[56:59], v[220:223], v[164:167], v[56:59]\n"
    "v_mfma_f32_16x16x32_bf16 v[60:63], v[216:219], v[168:171], v[60:63]\n"
    "v_mfma_f32_16x16x32_bf16 v[60:63], v[220:223], v[172:175], v[60:63]\n"
    "s_waitcnt lgkmcnt(9)\n"
    "v_mfma_f32_16x16x32_bf16 v[76:79], v[224:227], v[168:171], v[76:79]\n"
    "ds_read_b64_tr_b16 v[216:217], v236 offset:16384\n"
    "v_mfma_f32_16x16x32_bf16 v[76:79], v[228:231], v[172:175], v[76:79]\n"
    "ds_read_b64_tr_b16 v[218:219], v236 offset:17408\n"
    "v_mfma_f32_16x16x32_bf16 v[72:75], v[224:227], v[160:163], v[72:75]\n"
    "ds_read_b64_tr_b16 v[220:221], v236 offset:24576\n"
    "v_mfma_f32_16x16x32_bf16 v[72:75], v[228:231], v[164:167], v[72:75]\n"
    "ds_read_b64_tr_b16 v[222:223], v236 offset:25600\n"
    "v_mfma_f32_16x16x32_bf16 v[68:71], v[224:227], v[152:155], v[68:71]\n"
    "v_mfma_f32_16x16x32_bf16 v[68:71], v[228:231], v[156:159], v[68:71]\n"
    "v_mfma_f32_16x16x32_bf16 v[64:67], v[224:227], v[144:147], v[64:67]\n"
    "v_mfma_f32_16x16x32_bf16 v[64:67], v[228:231], v[148:151], v[64:67]\n"
    "v_add_u32 v237, 53248, v237\n"
    "s_waitcnt lgkmcnt(5)\n"
    "v_mfma_f32_16x16x32_bf16 v[16:19], v[208:211], v[176:179], v[16:19]\n"
    "ds_read_b64_tr_b16 v[224:225], v237 offset:0\n"
    "v_mfma_f32_16x16x32_bf16 v[16:19], v[212:215], v[180:183], v[16:19]\n"
    "ds_read_b64_tr_b16 v[226:227], v237 offset:1024\n"
    "v_mfma_f32_16x16x32_bf16 v[20:23], v[208:211], v[184:187], v[20:23]\n"
    "ds_read_b64_tr_b16 v[228:229], v237 offset:8192\n"
    "v_mfma_f32_16x16x32_bf16 v[20:23], v[212:215], v[188:191], v[20:23]\n"
    "ds_read_b64_tr_b16 v[230:231], v237 offset:9216\n"
    "v_mfma_f32_16x16x32_bf16 v[24:27], v[208:211], v[192:195], v[24:27]\n"
    "v_mfma_f32_16x16x32_bf16 v[24:27], v[212:215], v[196:199], v[24:27]\n"
    "v_mfma_f32_16x16x32_bf16 v[28:31], v[208:211], v[200:203], v[28:31]\n"
    "v_mfma_f32_16x16x32_bf16 v[28:31], v[212:215], v[204:207], v[28:31]\n"
    "s_waitcnt lgkmcnt(4)\n"
    "v_mfma_f32_16x16x32_bf16 v[44:47], v[216:219], v[200:203], v[44:47]\n"
    "ds_read_b64_tr_b16 v[208:209], v237 offset:16384\n"
    "v_mfma_f32_16x16x32_bf16 v[44:47], v[220:223], v[204:207], v[44:47]\n"
    "ds_read_b64_tr_b16 v[210:211], v237 offset:17408\n"
    "v_mfma_f32_16x16x32_bf16 v[40:43], v[216:219], v[192:195], v[40:43]\n"
    "ds_read_b64_tr_b16 v[212:213], v237 offset:24576\n"
    "v_mfma_f32_16x16x32_bf16 v[40:43], v[220:223], v[196:199], v[40:43]\n"
    "ds_read_b64_tr_b16 v[214:215], v237 offset:25600\n"
    "v_mfma_f32_16x16x32_bf16 v[36:39], v[216:219], v[184:187], v[36:39]\n"
    "v_mfma_f32_16x16x32_bf16 v[36:39], v[220:223], v[188:191], v[36:39]\n"
    "v_mfma_f32_16x16x32_bf16 v[32:35], v[216:219], v[176:179], v[32:35]\n"
    "s_nop 0\n"
    "s_barrier\n"
    "v_mfma_f32_16x16x32_bf16 v[32:35], v[220:223], v[180:183], v[32:35]\n"
    ".LPAD3_%=:\n"
    "s_cbranch_execz .LRET_%=\n"
    "s_nop 0\n"
    "s_waitcnt lgkmcnt(4)\n"
    "v_mfma_f32_16x16x32_bf16 v[48:51], v[224:227], v[176:179], v[48:51]\n"
    "ds_read_b64_tr_b16 v[216:217], v238 offset:0\n"
    "v_mfma_f32_16x16x32_bf16 v[48:51], v[228:231], v[180:183], v[48:51]\n"
    "ds_read_b64_tr_b16 v[218:219], v238 offset:1024\n"
    "v_mfma_f32_16x16x32_bf16 v[52:55], v[224:227], v[184:187], v[52:55]\n"
    "ds_read_b64_tr_b16 v[220:221], v238 offset:8192\n"
    "v_mfma_f32_16x16x32_bf16 v[52:55], v[228:231], v[188:191], v[52:55]\n"
    "ds_read_b64_tr_b16 v[222:223], v238 offset:9216\n"
    "v_mfma_f32_16x16x32_bf16 v[56:59], v[224:227], v[192:195], v[56:59]\n"
    "v_mfma_f32_16x16x32_bf16 v[56:59], v[228:231], v[196:199], v[56:59]\n"
    "v_mfma_f32_16x16x32_bf16 v[60:63], v[224:227], v[200:203], v[60:63]\n"
    "v_mfma_f32_16x16x32_bf16 v[60:63], v[228:231], v[204:207], v[60:63]\n"
    "v_mul_f32 v44, v44, v28\n"
    "v_mul_f32 v45, v45, v29\n"
    "s_waitcnt lgkmcnt(4)\n"
    "v_mfma_f32_16x16x32_bf16 v[76:79], v[208:211], v[200:203], v[76:79]\n"
    "ds_read_b64_tr_b16 v[224:225], v238 offset:16384\n"
    "v_mul_f32 v46, v46, v30\n"
    "v_mfma_f32_16x16x32_bf16 v[76:79], v[212:215], v[204:207], v[76:79]\n"
    "ds_read_b64_tr_b16 v[226:227], v238 offset:17408\n"
    "v_mul_f32 v47, v47, v31\n"
    "v_mfma_f32_16x16x32_bf16 v[72:75], v[208:211], v[192:195], v[72:75]\n"
    "ds_read_b64_tr_b16 v[228:229], v238 offset:24576\n"
    "v_mul_f32 v28, s84, v28\n"
    "v_mfma_f32_16x16x32_bf16 v[72:75], v[212:215], v[196:199], v[72:75]\n"
    "ds_read_b64_tr_b16 v[230:231], v238 offset:25600\n"
    "v_mul_f32 v29, s84, v29\n"
    "v_mfma_f32_16x16x32_bf16 v[68:71], v[208:211], v[184:187], v[68:71]\n"
    "v_mul_f32 v30, s84, v30\n"
    "v_mul_f32 v31, s84, v31\n"
    "v_mfma_f32_16x16x32_bf16 v[68:71], v[212:215], v[188:191], v[68:71]\n"
    "v_exp_f32 v28, v28\n"
    "v_mfma_f32_16x16x32_bf16 v[64:67], v[208:211], v[176:179], v[64:67]\n"
    "v_exp_f32 v29, v29\n"
    "v_mfma_f32_16x16x32_bf16 v[64:67], v[212:215], v[180:183], v[64:67]\n"
    "v_add_u32 v238, 53248, v238\n"
    "v_exp_f32 v30, v30\n"
    "s_waitcnt lgkmcnt(4)\n"
    "v_mfma_f32_16x16x32_bf16 v[80:83], v[216:219], v[144:147], v[80:83]\n"
    "ds_read_b64_tr_b16 v[208:209], v238 offset:0\n"
    "v_exp_f32 v31, v31\n"
    "v_mfma_f32_16x16x32_bf16 v[80:83], v[220:223], v[148:151], v[80:83]\n"
    "ds_read_b64_tr_b16 v[210:211], v238 offset:1024\n"
    "v_fma_f32 v28, v28, v251, v251\n"
    "v_mfma_f32_16x16x32_bf16 v[84:87], v[216:219], v[152:155], v[84:87]\n"
    "ds_read_b64_tr_b16 v[212:213], v238 offset:8192\n"
    "v_fma_f32 v29, v29, v251, v251\n"
    "v_mfma_f32_16x16x32_bf16 v[84:87], v[220:223], v[156:159], v[84:87]\n"
    "ds_read_b64_tr_b16 v[214:215], v238 offset:9216\n"
    "v_fma_f32 v30, v30, v251, v251\n"
    "v_mfma_f32_16x16x32_bf16 v[88:91], v[216:219], v[160:163], v[88:91]\n"
    "v_fma_f32 v31, v31, v251, v251\n"
    "v_rcp_f32 v28, v28\n"
    "v_mfma_f32_16x16x32_bf16 v[88:91], v[220:223], v[164:167], v[88:91]\n"
    "v_rcp_f32 v29, v29\n"
    "v_mfma_f32_16x16x32_bf16 v[92:95], v[216:219], v[168:171], v[92:95]\n"
    "v_rcp_f32 v30, v30\n"
    "v_mfma_f32_16x16x32_bf16 v[92:95], v[220:223], v[172:175], v[92:95]\n"
    "v_rcp_f32 v31, v31\n"
    "s_waitcnt lgkmcnt(4)\n"
    "v_mfma_f32_16x16x32_bf16 v[108:111], v[224:227], v[168:171], v[108:111]\n"
    "ds_read_b64_tr_b16 v[216:217], v238 offset:16384\n"
    "v_mul_f32 v44, v44, v28\n"
    "v_mfma_f32_16x16x32_bf16 v[108:111], v[228:231], v[172:175], v[108:111]\n"
    "ds_read_b64_tr_b16 v[218:219], v238 offset:17408\n"
    "v_mul_f32 v45, v45, v29\n"
    "v_mfma_f32_16x16x32_bf16 v[104:107], v[224:227], v[160:163], v[104:107]\n"
    "ds_read_b64_tr_b16 v[220:221], v238 offset:24576\n"
    "v_mul_f32 v46, v46, v30\n"
    "v_mfma_f32_16x16x32_bf16 v[104:107], v[228:231], v[164:167], v[104:107]\n"
    "ds_read_b64_tr_b16 v[222:223], v238 offset:25600\n"
    "v_mul_f32 v47, v47, v31\n"
    "v_mfma_f32_16x16x32_bf16 v[100:103], v[224:227], v[152:155], v[100:103]\n"
    "v_cvt_pk_bf16_f32 v12, v44, v45\n"
    "v_cvt_pk_bf16_f32 v13, v46, v47\n"
    "v_mfma_f32_16x16x32_bf16 v[100:103], v[228:231], v[156:159], v[100:103]\n"
    "v_mul_f32 v40, v40, v24\n"
    "v_mul_f32 v41, v41, v25\n"
    "v_mfma_f32_16x16x32_bf16 v[96:99], v[224:227], v[144:147], v[96:99]\n"
    "v_mul_f32 v42, v42, v26\n"
    "v_mul_f32 v43, v43, v27\n"
    "v_mfma_f32_16x16x32_bf16 v[96:99], v[228:231], v[148:151], v[96:99]\n"
    "v_mul_f32 v24, s84, v24\n"
    "v_mul_f32 v25, s84, v25\n"
    "s_waitcnt lgkmcnt(4)\n"
    "v_mfma_f32_16x16x32_bf16 v[80:83], v[208:211], v[176:179], v[80:83]\n"
    "ds_read_b64_tr_b16 v[224:225], v239 offset:0\n"
    "v_mul_f32 v26, s84, v26\n"
    "v_mfma_f32_16x16x32_bf16 v[80:83], v[212:215], v[180:183], v[80:83]\n"
    "ds_read_b64_tr_b16 v[226:227], v239 offset:1024\n"
    "v_mul_f32 v27, s84, v27\n"
    "v_mfma_f32_16x16x32_bf16 v[84:87], v[208:211], v[184:187], v[84:87]\n"
    "ds_read_b64_tr_b16 v[228:229], v239 offset:8192\n"
    "v_exp_f32 v24, v24\n"
    "v_mfma_f32_16x16x32_bf16 v[84:87], v[212:215], v[188:191], v[84:87]\n"
    "ds_read_b64_tr_b16 v[230:231], v239 offset:9216\n"
    "v_exp_f32 v25, v25\n"
    "v_mfma_f32_16x16x32_bf16 v[88:91], v[208:211], v[192:195], v[88:91]\n"
    "v_exp_f32 v26, v26\n"
    "v_mfma_f32_16x16x32_bf16 v[88:91], v[212:215], v[196:199], v[88:91]\n"
    "v_exp_f32 v27, v27\n"
    "v_mfma_f32_16x16x32_bf16 v[92:95], v[208:211], v[200:203], v[92:95]\n"
    "v_fma_f32 v24, v24, v250, v250\n"
    "v_fma_f32 v25, v25, v250, v250\n"
    "v_mfma_f32_16x16x32_bf16 v[92:95], v[212:215], v[204:207], v[92:95]\n"
    "v_fma_f32 v26, v26, v250, v250\n"
    "v_fma_f32 v27, v27, v250, v250\n"
    "s_waitcnt lgkmcnt(4)\n"
    "v_mfma_f32_16x16x32_bf16 v[108:111], v[216:219], v[200:203], v[108:111]\n"
    "ds_read_b64_tr_b16 v[208:209], v239 offset:16384\n"
    "v_rcp_f32 v24, v24\n"
    "v_mfma_f32_16x16x32_bf16 v[108:111], v[220:223], v[204:207], v[108:111]\n"
    "ds_read_b64_tr_b16 v[210:211], v239 offset:17408\n"
    "v_rcp_f32 v25, v25\n"
    "v_mfma_f32_16x16x32_bf16 v[104:107], v[216:219], v[192:195], v[104:107]\n"
    "ds_read_b64_tr_b16 v[212:213], v239 offset:24576\n"
    "v_rcp_f32 v26, v26\n"
    "v_mfma_f32_16x16x32_bf16 v[104:107], v[220:223], v[196:199], v[104:107]\n"
    "ds_read_b64_tr_b16 v[214:215], v239 offset:25600\n"
    "v_rcp_f32 v27, v27\n"
    "v_mfma_f32_16x16x32_bf16 v[100:103], v[216:219], v[184:187], v[100:103]\n"
    "v_mul_f32 v40, v40, v24\n"
    "v_mul_f32 v41, v41, v25\n"
    "v_mfma_f32_16x16x32_bf16 v[100:103], v[220:223], v[188:191], v[100:103]\n"
    "v_mul_f32 v42, v42, v26\n"
    "v_mul_f32 v43, v43, v27\n"
    "v_mfma_f32_16x16x32_bf16 v[96:99], v[216:219], v[176:179], v[96:99]\n"
    "v_cvt_pk_bf16_f32 v8, v40, v41\n"
    "v_cvt_pk_bf16_f32 v9, v42, v43\n"
    "v_mfma_f32_16x16x32_bf16 v[96:99], v[220:223], v[180:183], v[96:99]\n"
    "v_add_u32 v239, 53248, v239\n"
    "v_mul_f32 v36, v36, v20\n"
    "v_mul_f32 v37, v37, v21\n"
    "s_waitcnt lgkmcnt(4)\n"
    "v_mfma_f32_16x16x32_bf16 v[112:115], v[224:227], v[144:147], v[112:115]\n"
    "ds_read_b64_tr_b16 v[216:217], v239 offset:0\n"
    "v_mul_f32 v38, v38, v22\n"
    "v_mfma_f32_16x16x32_bf16 v[112:115], v[228:231], v[148:151], v[112:115]\n"
    "ds_read_b64_tr_b16 v[218:219], v239 offset:1024\n"
    "v_mul_f32 v39, v39, v23\n"
    "v_mfma_f32_16x16x32_bf16 v[116:119], v[224:227], v[152:155], v[116:119]\n"
    "ds_read_b64_tr_b16 v[220:221], v239 offset:8192\n"
    "v_mul_f32 v20, s84, v20\n"
    "v_mfma_f32_16x16x32_bf16 v[116:119], v[228:231], v[156:159], v[116:119]\n"
    "ds_read_b64_tr_b16 v[222:223], v239 offset:9216\n"
    "v_mul_f32 v21, s84, v21\n"
    "v_mfma_f32_16x16x32_bf16 v[120:123], v[224:227], v[160:163], v[120:123]\n"
    "v_mul_f32 v22, s84, v22\n"
    "v_mul_f32 v23, s84, v23\n"
    "v_mfma_f32_16x16x32_bf16 v[120:123], v[228:231], v[164:167], v[120:123]\n"
    "v_exp_f32 v20, v20\n"
    "v_mfma_f32_16x16x32_bf16 v[124:127], v[224:227], v[168:171], v[124:127]\n"
    "v_exp_f32 v21, v21\n"
    "v_mfma_f32_16x16x32_bf16 v[124:127], v[228:231], v[172:175], v[124:127]\n"
    "v_exp_f32 v22, v22\n"
    "s_waitcnt lgkmcnt(4)\n"
    "v_mfma_f32_16x16x32_bf16 v[140:143], v[208:211], v[168:171], v[140:143]\n"
    "ds_read_b64_tr_b16 v[224:225], v239 offset:16384\n"
    "v_exp_f32 v23, v23\n"
    "v_mfma_f32_16x16x32_bf16 v[140:143], v[212:215], v[172:175], v[140:143]\n"
    "ds_read_b64_tr_b16 v[226:227], v239 offset:17408\n"
    "v_fma_f32 v20, v20, v249, v249\n"
    "v_mfma_f32_16x16x32_bf16 v[136:139], v[208:211], v[160:163], v[136:139]\n"
    "ds_read_b64_tr_b16 v[228:229], v239 offset:24576\n"
    "v_fma_f32 v21, v21, v249, v249\n"
    "v_mfma_f32_16x16x32_bf16 v[136:139], v[212:215], v[164:167], v[136:139]\n"
    "ds_read_b64_tr_b16 v[230:231], v239 offset:25600\n"
    "v_fma_f32 v22, v22, v249, v249\n"
    "v_mfma_f32_16x16x32_bf16 v[132:135], v[208:211], v[152:155], v[132:135]\n"
    "v_fma_f32 v23, v23, v249, v249\n"
    "v_rcp_f32 v20, v20\n"
    "v_mfma_f32_16x16x32_bf16 v[132:135], v[212:215], v[156:159], v[132:135]\n"
    "v_rcp_f32 v21, v21\n"
    "s_cmp_eq_u32 s74, 0\n"
    "v_mfma_f32_16x16x32_bf16 v[128:131], v[208:211], v[144:147], v[128:131]\n"
    "v_rcp_f32 v22, v22\n"
    "s_waitcnt vmcnt(0) lgkmcnt(0)\n"
    "s_barrier\n"
    "s_cbranch_scc0 .LAF5_%=\n"
    "s_mov_b32 exec_lo, 65535\n"
    "s_mov_b32 exec_hi, 0\n"
    "buffer_store_dwordx2 v[252:253], v243, s[60:63], 0 offen sc1\n"
    "s_mov_b64 exec, -1\n"
    ".LAF5_%=:\n"
    "v_mfma_f32_16x16x32_bf16 v[128:131], v[212:215], v[148:151], v[128:131]\n"
    "v_rcp_f32 v23, v23\n"
    "v_mfma_f32_16x16x32_bf16 v[112:115], v[216:219], v[176:179], v[112:115]\n"
    "v_mul_f32 v36, v36, v20\n"
    "v_mul_f32 v37, v37, v21\n"
    "v_mfma_f32_16x16x32_bf16 v[112:115], v[220:223], v[180:183], v[112:115]\n"
    "v_mul_f32 v38, v38, v22\n"
    "v_mul_f32 v39, v39, v23\n"
    "v_mfma_f32_16x16x32_bf16 v[116:119], v[216:219], v[184:187], v[116:119]\n"
    "v_cvt_pk_bf16_f32 v4, v36, v37\n"
    "v_cvt_pk_bf16_f32 v5, v38, v39\n"
    "v_mfma_f32_16x16x32_bf16 v[116:119], v[220:223], v[188:191], v[116:119]\n"
    "v_mul_f32 v32, v32, v16\n"
    "v_mul_f32 v33, v33, v17\n"
    "v_mfma_f32_16x16x32_bf16 v[120:123], v[216:219], v[192:195], v[120:123]\n"
    "v_mul_f32 v34, v34, v18\n"
    "v_mul_f32 v35, v35, v19\n"
    "v_mfma_f32_16x16x32_bf16 v[120:123], v[220:223], v[196:199], v[120:123]\n"
    "v_mul_f32 v16, s84, v16\n"
    "v_mul_f32 v17, s84, v17\n"
    "v_mfma_f32_16x16x32_bf16 v[124:127], v[216:219], v[200:203], v[124:127]\n"
    "v_mul_f32 v18, s84, v18\n"
    "v_mul_f32 v19, s84, v19\n"
    "v_mfma_f32_16x16x32_bf16 v[124:127], v[220:223], v[204:207], v[124:127]\n"
    "v_exp_f32 v16, v16\n"
    "v_mfma_f32_16x16x32_bf16 v[140:143], v[224:227], v[200:203], v[140:143]\n"
    "v_exp_f32 v17, v17\n"
    "v_mfma_f32_16x16x32_bf16 v[140:143], v[228:231], v[204:207], v[140:143]\n"
    "v_exp_f32 v18, v18\n"
    "v_mfma_f32_16x16x32_bf16 v[136:139], v[224:227], v[192:195], v[136:139]\n"
    "v_exp_f32 v19, v19\n"
    "v_mfma_f32_16x16x32_bf16 v[136:139], v[228:231], v[196:199], v[136:139]\n"
    "v_fma_f32 v16, v16, v248, v248\n"
    "v_fma_f32 v17, v17, v248, v248\n"
    "v_mfma_f32_16x16x32_bf16 v[132:135], v[224:227], v[184:187], v[132:135]\n"
    "v_fma_f32 v18, v18, v248, v248\n"
    "v_fma_f32 v19, v19, v248, v248\n"
    "v_mfma_f32_16x16x32_bf16 v[132:135], v[228:231], v[188:191], v[132:135]\n"
    "v_rcp_f32 v16, v16\n"
    "v_mfma_f32_16x16x32_bf16 v[128:131], v[224:227], v[176:179], v[128:131]\n"
    "v_rcp_f32 v17, v17\n"
    "v_mfma_f32_16x16x32_bf16 v[128:131], v[228:231], v[180:183], v[128:131]\n"
    "v_rcp_f32 v18, v18\n"
    "s_waitcnt lgkmcnt(0)\n"
    "v_rcp_f32 v19, v19\n"
    "v_mul_f32 v32, v32, v16\n"
    "v_mul_f32 v33, v33, v17\n"
    "v_mul_f32 v34, v34, v18\n"
    "v_mul_f32 v35, v35, v19\n"
    "v_cvt_pk_bf16_f32 v0, v32, v33\n"
    "v_cvt_pk_bf16_f32 v1, v34, v35\n"
    "s_nop 7\n"
    "v_pk_mul_f32 v[76:77], v[76:77], v[60:61]\n"
    "v_pk_mul_f32 v[72:73], v[72:73], v[56:57]\n"
    "v_pk_mul_f32 v[78:79], v[78:79], v[62:63]\n"
    "v_pk_mul_f32 v[74:75], v[74:75], v[58:59]\n"
    "v_pk_mul_f32 v[60:61], v[60:61], s[84:85]\n"
    "v_pk_mul_f32 v[56:57], v[56:57], s[84:85]\n"
    "v_pk_mul_f32 v[62:63], v[62:63], s[84:85]\n"
    "v_pk_mul_f32 v[58:59], v[58:59], s[84:85]\n"
    "v_exp_f32 v60, v60\n"
    "v_exp_f32 v56, v56\n"
    "v_exp_f32 v61, v61\n"
    "v_exp_f32 v57, v57\n"
    "v_exp_f32 v62, v62\n"
    "v_exp_f32 v58, v58\n"
    "v_exp_f32 v63, v63\n"
    "v_exp_f32 v59, v59\n"
    "v_pk_fma_f32 v[60:61], v[60:61], v[250:251], v[250:251] op_sel:[0,1,1] op_sel_hi:[1,1,1]\n"
    "v_pk_fma_f32 v[56:57], v[56:57], v[250:251], v[250:251] op_sel:[0,0,0] op_sel_hi:[1,0,0]\n"
    "v_pk_fma_f32 v[62:63], v[62:63], v[250:251], v[250:251] op_sel:[0,1,1] op_sel_hi:[1,1,1]\n"
    "v_pk_fma_f32 v[58:59], v[58:59], v[250:251], v[250:251] op_sel:[0,0,0] op_sel_hi:[1,0,0]\n"
    "v_rcp_f32 v60, v60\n"
    "v_rcp_f32 v56, v56\n"
    "v_rcp_f32 v61, v61\n"
    "v_rcp_f32 v57, v57\n"
    "v_rcp_f32 v62, v62\n"
    "v_rcp_f32 v58, v58\n"
    "v_rcp_f32 v63, v63\n"
    "v_rcp_f32 v59, v59\n"
    "v_pk_mul_f32 v[76:77], v[76:77], v[60:61]\n"
    "v_pk_mul_f32 v[72:73], v[72:73], v[56:57]\n"
    "v_pk_mul_f32 v[78:79], v[78:79], v[62:63]\n"
    "v_pk_mul_f32 v[74:75], v[74:75], v[58:59]\n"
    "v_cvt_pk_bf16_f32 v14, v76, v77\n"
    "v_cvt_pk_bf16_f32 v10, v72, v73\n"
    "v_cvt_pk_bf16_f32 v15, v78, v79\n"
    "v_cvt_pk_bf16_f32 v11, v74, v75\n"
    "v_pk_mul_f32 v[68:69], v[68:69], v[52:53]\n"
    "v_pk_mul_f32 v[64:65], v[64:65], v[48:49]\n"
    "v_pk_mul_f32 v[70:71], v[70:71], v[54:55]\n"
    "v_pk_mul_f32 v[66:67], v[66:67], v[50:51]\n"
    "v_pk_mul_f32 v[52:53], v[52:53], s[84:85]\n"
    "v_pk_mul_f32 v[48:49], v[48:49], s[84:85]\n"
    "v_pk_mul_f32 v[54:55], v[54:55], s[84:85]\n"
    "v_pk_mul_f32 v[50:51], v[50:51], s[84:85]\n"
    "v_exp_f32 v52, v52\n"
    "v_exp_f32 v48, v48\n"
    "v_exp_f32 v53, v53\n"
    "v_exp_f32 v49, v49\n"
    "v_exp_f32 v54, v54\n"
    "v_exp_f32 v50, v50\n"
    "v_exp_f32 v55, v55\n"
    "v_exp_f32 v51, v51\n"
    "v_pk_fma_f32 v[52:53], v[52:53], v[248:249], v[248:249] op_sel:[0,1,1] op_sel_hi:[1,1,1]\n"
    "v_pk_fma_f32 v[48:49], v[48:49], v[248:249], v[248:249] op_sel:[0,0,0] op_sel_hi:[1,0,0]\n"
    "v_pk_fma_f32 v[54:55], v[54:55], v[248:249], v[248:249] op_sel:[0,1,1] op_sel_hi:[1,1,1]\n"
    "v_pk_fma_f32 v[50:51], v[50:51], v[248:249], v[248:249] op_sel:[0,0,0] op_sel_hi:[1,0,0]\n"
    "v_rcp_f32 v52, v52\n"
    "v_rcp_f32 v48, v48\n"
    "v_rcp_f32 v53, v53\n"
    "v_rcp_f32 v49, v49\n"
    "v_rcp_f32 v54, v54\n"
    "v_rcp_f32 v50, v50\n"
    "v_rcp_f32 v55, v55\n"
    "v_rcp_f32 v51, v51\n"
    "v_pk_mul_f32 v[68:69], v[68:69], v[52:53]\n"
    "v_pk_mul_f32 v[64:65], v[64:65], v[48:49]\n"
    "v_pk_mul_f32 v[70:71], v[70:71], v[54:55]\n"
    "v_pk_mul_f32 v[66:67], v[66:67], v[50:51]\n"
    "v_cvt_pk_bf16_f32 v6, v68, v69\n"
    "v_cvt_pk_bf16_f32 v2, v64, v65\n"
    "v_cvt_pk_bf16_f32 v7, v70, v71\n"
    "v_cvt_pk_bf16_f32 v3, v66, v67\n"
    "v_pk_mul_f32 v[108:109], v[108:109], v[92:93]\n"
    "v_pk_mul_f32 v[104:105], v[104:105], v[88:89]\n"
    "v_pk_mul_f32 v[110:111], v[110:111], v[94:95]\n"
    "v_pk_mul_f32 v[106:107], v[106:107], v[90:91]\n"
    "v_pk_mul_f32 v[92:93], v[92:93], s[84:85]\n"
    "v_pk_mul_f32 v[88:89], v[88:89], s[84:85]\n"
    "v_pk_mul_f32 v[94:95], v[94:95], s[84:85]\n"
    "v_pk_mul_f32 v[90:91], v[90:91], s[84:85]\n"
    "v_exp_f32 v92, v92\n"
    "v_exp_f32 v88, v88\n"
    "v_exp_f32 v93, v93\n"
    "v_exp_f32 v89, v89\n"
    "v_exp_f32 v94, v94\n"
    "v_exp_f32 v90, v90\n"
    "v_exp_f32 v95, v95\n"
    "v_exp_f32 v91, v91\n"
    "v_pk_fma_f32 v[92:93], v[92:93], v[250:251], v[250:251] op_sel:[0,1,1] op_sel_hi:[1,1,1]\n"
    "v_pk_fma_f32 v[88:89], v[88:89], v[250:251], v[250:251] op_sel:[0,0,0] op_sel_hi:[1,0,0]\n"
    "v_pk_fma_f32 v[94:95], v[94:95], v[250:251], v[250:251] op_sel:[0,1,1] op_sel_hi:[1,1,1]\n"
    "v_pk_fma_f32 v[90:91], v[90:91], v[250:251], v[250:251] op_sel:[0,0,0] op_sel_hi:[1,0,0]\n"
    "v_rcp_f32 v92, v92\n"
    "v_rcp_f32 v88, v88\n"
    "v_rcp_f32 v93, v93\n"
    "v_rcp_f32 v89, v89\n"
    "v_rcp_f32 v94, v94\n"
    "v_rcp_f32 v90, v90\n"
    "v_rcp_f32 v95, v95\n"
    "v_rcp_f32 v91, v91\n"
    "v_pk_mul_f32 v[108:109], v[108:109], v[92:93]\n"
    "v_pk_mul_f32 v[104:105], v[104:105], v[88:89]\n"
    "v_pk_mul_f32 v[110:111], v[110:111], v[94:95]\n"
    "v_pk_mul_f32 v[106:107], v[106:107], v[90:91]\n"
    "v_cvt_pk_bf16_f32 v28, v108, v109\n"
    "v_cvt_pk_bf16_f32 v24, v104, v105\n"
    "v_cvt_pk_bf16_f32 v29, v110, v111\n"
    "v_cvt_pk_bf16_f32 v25, v106, v107\n"
    "v_pk_mul_f32 v[100:101], v[100:101], v[84:85]\n"
    "v_pk_mul_f32 v[96:97], v[96:97], v[80:81]\n"
    "v_pk_mul_f32 v[102:103], v[102:103], v[86:87]\n"
    "v_pk_mul_f32 v[98:99], v[98:99], v[82:83]\n"
    "v_pk_mul_f32 v[84:85], v[84:85], s[84:85]\n"
    "v_pk_mul_f32 v[80:81], v[80:81], s[84:85]\n"
    "v_pk_mul_f32 v[86:87], v[86:87], s[84:85]\n"
    "v_pk_mul_f32 v[82:83], v[82:83], s[84:85]\n"
    "v_exp_f32 v84, v84\n"
    "v_exp_f32 v80, v80\n"
    "v_exp_f32 v85, v85\n"
    "v_exp_f32 v81, v81\n"
    "v_exp_f32 v86, v86\n"
    "v_exp_f32 v82, v82\n"
    "v_exp_f32 v87, v87\n"
    "v_exp_f32 v83, v83\n"
    "v_pk_fma_f32 v[84:85], v[84:85], v[248:249], v[248:249] op_sel:[0,1,1] op_sel_hi:[1,1,1]\n"
    "v_pk_fma_f32 v[80:81], v[80:81], v[248:249], v[248:249] op_sel:[0,0,0] op_sel_hi:[1,0,0]\n"
    "v_pk_fma_f32 v[86:87], v[86:87], v[248:249], v[248:249] op_sel:[0,1,1] op_sel_hi:[1,1,1]\n"
    "v_pk_fma_f32 v[82:83], v[82:83], v[248:249], v[248:249] op_sel:[0,0,0] op_sel_hi:[1,0,0]\n"
    "v_rcp_f32 v84, v84\n"
    "v_rcp_f32 v80, v80\n"
    "v_rcp_f32 v85, v85\n"
    "v_rcp_f32 v81, v81\n"
    "v_rcp_f32 v86, v86\n"
    "v_rcp_f32 v82, v82\n"
    "v_rcp_f32 v87, v87\n"
    "v_rcp_f32 v83, v83\n"
    "v_pk_mul_f32 v[100:101], v[100:101], v[84:85]\n"
    "v_pk_mul_f32 v[96:97], v[96:97], v[80:81]\n"
    "v_pk_mul_f32 v[102:103], v[102:103], v[86:87]\n"
    "v_pk_mul_f32 v[98:99], v[98:99], v[82:83]\n"
    "v_cvt_pk_bf16_f32 v20, v100, v101\n"
    "v_cvt_pk_bf16_f32 v16, v96, v97\n"
    "v_cvt_pk_bf16_f32 v21, v102, v103\n"
    "v_cvt_pk_bf16_f32 v17, v98, v99\n"
    "v_pk_mul_f32 v[140:141], v[140:141], v[124:125]\n"
    "v_pk_mul_f32 v[136:137], v[136:137], v[120:121]\n"
    "v_pk_mul_f32 v[142:143], v[142:143], v[126:127]\n"
    "v_pk_mul_f32 v[138:139], v[138:139], v[122:123]\n"
    "v_pk_mul_f32 v[124:125], v[124:125], s[84:85]\n"
    "v_pk_mul_f32 v[120:121], v[120:121], s[84:85]\n"
    "v_pk_mul_f32 v[126:127], v[126:127], s[84:85]\n"
    "v_pk_mul_f32 v[122:123], v[122:123], s[84:85]\n"
    "v_exp_f32 v124, v124\n"
    "v_exp_f32 v120, v120\n"
    "v_exp_f32 v125, v125\n"
    "v_exp_f32 v121, v121\n"
    "v_exp_f32 v126, v126\n"
    "v_exp_f32 v122, v122\n"
    "v_exp_f32 v127, v127\n"
    "v_exp_f32 v123, v123\n"
    "v_pk_fma_f32 v[124:125], v[124:125], v[250:251], v[250:251] op_sel:[0,1,1] op_sel_hi:[1,1,1]\n"
    "v_pk_fma_f32 v[120:121], v[120:121], v[250:251], v[250:251] op_sel:[0,0,0] op_sel_hi:[1,0,0]\n"
    "v_pk_fma_f32 v[126:127], v[126:127], v[250:251], v[250:251] op_sel:[0,1,1] op_sel_hi:[1,1,1]\n"
    "v_pk_fma_f32 v[122:123], v[122:123], v[250:251], v[250:251] op_sel:[0,0,0] op_sel_hi:[1,0,0]\n"
    "v_rcp_f32 v124, v124\n"
    "v_rcp_f32 v120, v120\n"
    "v_rcp_f32 v125, v125\n"
    "v_rcp_f32 v121, v121\n"
    "v_rcp_f32 v126, v126\n"
    "v_rcp_f32 v122, v122\n"
    "v_rcp_f32 v127, v127\n"
    "v_rcp_f32 v123, v123\n"
    "v_pk_mul_f32 v[140:141], v[140:141], v[124:125]\n"
    "v_pk_mul_f32 v[136:137], v[136:137], v[120:121]\n"
    "v_pk_mul_f32 v[142:143], v[142:143], v[126:127]\n"
    "v_pk_mul_f32 v[138:139], v[138:139], v[122:123]\n"
    "v_cvt_pk_bf16_f32 v30, v140, v141\n"
    "v_cvt_pk_bf16_f32 v26, v136, v137\n"
    "v_cvt_pk_bf16_f32 v31, v142, v143\n"
    "v_cvt_pk_bf16_f32 v27, v138, v139\n"
    "v_pk_mul_f32 v[132:133], v[132:133], v[116:117]\n"
    "v_pk_mul_f32 v[128:129], v[128:129], v[112:113]\n"
    "v_pk_mul_f32 v[134:135], v[134:135], v[118:119]\n"
    "v_pk_mul_f32 v[130:131], v[130:131], v[114:115]\n"
    "v_pk_mul_f32 v[116:117], v[116:117], s[84:85]\n"
    "v_pk_mul_f32 v[112:113], v[112:113], s[84:85]\n"
    "v_pk_mul_f32 v[118:119], v[118:119], s[84:85]\n"
    "v_pk_mul_f32 v[114:115], v[114:115], s[84:85]\n"
    "v_exp_f32 v116, v116\n"
    "v_exp_f32 v112, v112\n"
    "v_exp_f32 v117, v117\n"
    "v_exp_f32 v113, v113\n"
    "v_exp_f32 v118, v118\n"
    "v_exp_f32 v114, v114\n"
    "v_exp_f32 v119, v119\n"
    "v_exp_f32 v115, v115\n"
    "v_pk_fma_f32 v[116:117], v[116:117], v[248:249], v[248:249] op_sel:[0,1,1] op_sel_hi:[1,1,1]\n"
    "v_pk_fma_f32 v[112:113], v[112:113], v[248:249], v[248:249] op_sel:[0,0,0] op_sel_hi:[1,0,0]\n"
    "v_pk_fma_f32 v[118:119], v[118:119], v[248:249], v[248:249] op_sel:[0,1,1] op_sel_hi:[1,1,1]\n"
    "v_pk_fma_f32 v[114:115], v[114:115], v[248:249], v[248:249] op_sel:[0,0,0] op_sel_hi:[1,0,0]\n"
    "v_rcp_f32 v116, v116\n"
    "v_rcp_f32 v112, v112\n"
    "v_rcp_f32 v117, v117\n"
    "v_rcp_f32 v113, v113\n"
    "v_rcp_f32 v118, v118\n"
    "v_rcp_f32 v114, v114\n"
    "v_rcp_f32 v119, v119\n"
    "v_rcp_f32 v115, v115\n"
    "v_pk_mul_f32 v[132:133], v[132:133], v[116:117]\n"
    "v_pk_mul_f32 v[128:129], v[128:129], v[112:113]\n"
    "v_pk_mul_f32 v[134:135], v[134:135], v[118:119]\n"
    "v_pk_mul_f32 v[130:131], v[130:131], v[114:115]\n"
    "v_cvt_pk_bf16_f32 v22, v132, v133\n"
    "v_cvt_pk_bf16_f32 v18, v128, v129\n"
    "v_cvt_pk_bf16_f32 v23, v134, v135\n"
    "v_cvt_pk_bf16_f32 v19, v130, v131\n"
    "s_branch .LA3_%=\n"
    ".fill 309, 4, 0xbf800000\n"
    ".LAZ_%=:\n"
    "v_lshrrev_b32 v241, 4, v240\n"
    "v_and_b32 v242, 15, v240\n"
    "v_lshlrev_b32 v247, 4, v240\n"
    "v_lshrrev_b32 v243, 1, v242\n"
    "v_xor_b32 v243, v243, v241\n"
    "v_lshlrev_b32 v243, 4, v243\n"
    "v_lshl_add_u32 v243, v242, 7, v243\n"
    "s_lshl_b32 s71, s75, 13\n"
    "v_add_u32 v232, s71, v243\n"
    "v_xor_b32 v233, 64, v232\n"
    "v_lshrrev_b32 v244, 2, v242\n"
    "v_lshl_add_u32 v245, v241, 3, v244\n"
    "v_and_b32 v246, 3, v240\n"
    "v_lshlrev_b32 v246, 3, v246\n"
    "s_lshl_b32 s71, s75, 8\n"
    "s_add_u32 s71, s71, 106496\n"
    "v_lshl_add_u32 v254, v242, 2, s71\n"
    "s_mov_b32 s83, 4294860800\n"
    "s_mov_b32 s81, 0\n"
    "s_mov_b32 s80, 0\n"
    "s_mov_b32 s84, 0xbfb8aa3b\n"
    "s_mov_b32 s85, 0xbfb8aa3b\n"
    "v_mov_b64 v[0:1], 0\n"
    "v_mov_b64 v[2:3], 0\n"
    "v_mov_b64 v[4:5], 0\n"
    "v_mov_b64 v[6:7], 0\n"
    "v_mov_b64 v[8:9], 0\n"
    "v_mov_b64 v[10:11], 0\n"
    "v_mov_b64 v[12:13], 0\n"
    "v_mov_b64 v[14:15], 0\n"
    "v_mov_b64 v[16:17], 0\n"
    "v_mov_b64 v[18:19], 0\n"
    "v_mov_b64 v[20:21], 0\n"
    "v_mov_b64 v[22:23], 0\n"
    "v_mov_b64 v[24:25], 0\n"
    "v_mov_b64 v[26:27], 0\n"
    "v_mov_b64 v[28:29], 0\n"
    "v_mov_b64 v[30:31], 0\n"
    "v_mov_b64 v[32:33], 0\n"
    "v_mov_b64 v[34:35], 0\n"
    "v_mov_b64 v[36:37], 0\n"
    "v_mov_b64 v[38:39], 0\n"
    "v_mov_b64 v[40:41], 0\n"
    "v_mov_b64 v[42:43], 0\n"
    "v_mov_b64 v[44:45], 0\n"
    "v_mov_b64 v[46:47], 0\n"
    "v_mov_b64 v[48:49], 0\n"
    "v_mov_b64 v[50:51], 0\n"
    "v_mov_b64 v[52:53], 0\n"
    "v_mov_b64 v[54:55], 0\n"
    "v_mov_b64 v[56:57], 0\n"
    "v_mov_b64 v[58:59], 0\n"
    "v_mov_b64 v[60:61], 0\n"
    "v_mov_b64 v[62:63], 0\n"
    "v_mov_b64 v[64:65], 0\n"
    "v_mov_b64 v[66:67], 0\n"
    "v_mov_b64 v[68:69], 0\n"
    "v_mov_b64 v[70:71], 0\n"
    "v_mov_b64 v[72:73], 0\n"
    "v_mov_b64 v[74:75], 0\n"
    "v_mov_b64 v[76:77], 0\n"
    "v_mov_b64 v[78:79], 0\n"
    "v_mov_b64 v[80:81], 0\n"
    "v_mov_b64 v[82:83], 0\n"
    "v_mov_b64 v[84:85], 0\n"
    "v_mov_b64 v[86:87], 0\n"
    "v_mov_b64 v[88:89], 0\n"
    "v_mov_b64 v[90:91], 0\n"
    "v_mov_b64 v[92:93], 0\n"
    "v_mov_b64 v[94:95], 0\n"
    "v_mov_b64 v[96:97], 0\n"
    "v_mov_b64 v[98:99], 0\n"
    "v_mov_b64 v[100:101], 0\n"
    "v_mov_b64 v[102:103], 0\n"
    "v_mov_b64 v[104:105], 0\n"
    "v_mov_b64 v[106:107], 0\n"
    "v_mov_b64 v[108:109], 0\n"
    "v_mov_b64 v[110:111], 0\n"
    "v_mov_b64 v[112:113], 0\n"
    "v_mov_b64 v[114:115], 0\n"
    "v_mov_b64 v[116:117], 0\n"
    "v_mov_b64 v[118:119], 0\n"
    "v_mov_b64 v[120:121], 0\n"
    "v_mov_b64 v[122:123], 0\n"
    "v_mov_b64 v[124:125], 0\n"
    "v_mov_b64 v[126:127], 0\n"
    "v_mov_b64 v[128:129], 0\n"
    "v_mov_b64 v[130:131], 0\n"
    "v_mov_b64 v[132:133], 0\n"
    "v_mov_b64 v[134:135], 0\n"
    "v_mov_b64 v[136:137], 0\n"
    "v_mov_b64 v[138:139], 0\n"
    "v_mov_b64 v[140:141], 0\n"
    "v_mov_b64 v[142:143], 0\n"
    "s_xor_b32 s77, s70, s35\n"
    "s_and_b32 s77, s77, 3\n"
    "s_lshl_b32 s91, s74, 12\n"
    "s_waitcnt lgkmcnt(0)\n"
    "s_mov_b32 s54, 8388608\n"
    "s_mov_b32 s55, 0x20000\n"
    "s_cmp_ge_u32 s34, 2\n"
    "s_cselect_b64 s[52:53], s[12:13], s[10:11]\n"
    "s_and_b32 s53, s53, 0xffff\n"
    "s_mov_b32 s58, 1835008\n"
    "s_mov_b32 s59, 0x20000\n"
    "s_mov_b32 s56, s22\n"
    "s_and_b32 s57, s23, 0xffff\n"
    "s_mov_b32 s62, 25165824\n"
    "s_mov_b32 s63, 0x20000\n"
    "s_mov_b32 s60, s28\n"
    "s_and_b32 s61, s29, 0xffff\n"
    "s_mov_b32 s66, 2097152\n"
    "s_mov_b32 s67, 0x20000\n"
    "s_mov_b32 s64, s26\n"
    "s_and_b32 s65, s27, 0xffff\n"
    "s_mul_i32 s71, s76, 10240\n"
    "s_add_u32 s71, s71, 32768\n"
    "v_add_u32 v234, s71, v243\n"
    "v_xor_b32 v235, 64, v234\n"
    "v_and_b32 v236, 1, v241\n"
    "v_xor_b32 v236, s76, v236\n"
    "v_lshrrev_b32 v244, 1, v244\n"
    "v_lshl_add_u32 v236, v236, 1, v244\n"
    "v_lshl_add_u32 v236, v236, 5, v246\n"
    "v_lshl_add_u32 v236, v245, 7, v236\n"
    "v_add_u32 v236, 16384, v236\n"
    "v_xor_b32 v237, 32, v236\n"
    "s_lshr_b32 s71, s34, 1\n"
    "s_lshl4_add_u32 s71, s71, s70\n"
    "s_lshl1_add_u32 s71, s71, s75\n"
    "s_and_b32 s72, s34, 1\n"
    "s_lshl1_add_u32 s71, s71, s72\n"
    "s_lshl3_add_u32 s71, s71, s35\n"
    "s_mul_i32 s72, s76, 5\n"
    "s_lshl1_add_u32 s71, s71, s72\n"
    "s_lshl_b32 s93, s71, 11\n"
    "s_add_u32 s94, s93, 4096\n"
    "s_add_u32 s95, s93, 8192\n"
    "s_lshl2_add_u32 s71, s70, s34\n"
    "s_mul_i32 s71, s71, 7\n"
    "s_lshl1_add_u32 s71, s35, s71\n"
    "s_lshl1_add_u32 s71, s71, s75\n"
    "s_lshl3_add_u32 s71, s71, s76\n"
    "s_lshl_b32 s71, s71, 10\n"
    "s_add_u32 s96, s71, 4194304\n"
    "s_add_u32 s97, s71, 4198400\n"
    "v_and_b32 v243, 7, v240\n"
    "v_bfe_u32 v244, v240, 3, 2\n"
    "v_lshl_add_u32 v243, v243, 3, v244\n"
    "s_and_b32 s71, s34, 1\n"
    "s_lshl_b32 s71, s71, 4\n"
    "s_and_b32 s72, s34, 2\n"
    "s_add_u32 s71, s71, s72\n"
    "s_lshr_b32 s72, s70, 3\n"
    "s_add_u32 s71, s71, s72\n"
    "s_lshl2_add_u32 s71, s71, s35\n"
    "s_and_b32 s72, s70, 7\n"
    "s_lshl4_add_u32 s71, s71, s72\n"
    "s_lshl3_add_u32 s71, s71, 20971520\n"
    "v_lshl_add_u32 v243, v243, 11, s71\n"
    "v_xor_b32 v243, 12288, v243\n"
    "s_waitcnt vmcnt(0)\n"
    "s_mov_b64 exec, 0\n"
    "s_cmp_eq_u32 s74, 0\n"
    "s_cbranch_scc0 .LW40_%=\n"
    "s_call_b64 s[18:19], .LPAD0_%=\n"
    ".LW40_%=:\n"
    "s_cmp_eq_u32 s74, 1\n"
    "s_cbranch_scc0 .LW41_%=\n"
    "s_call_b64 s[18:19], .LPAD4_%=\n"
    ".LW41_%=:\n"
    "s_cmp_eq_u32 s74, 2\n"
    "s_cbranch_scc0 .LW42_%=\n"
    "s_nop 0\n"
    ".LW42_%=:\n"
    "s_cmp_eq_u32 s74, 3\n"
    "s_cbranch_scc0 .LW43_%=\n"
    "s_nop 0\n"
    ".LW43_%=:\n"
    "s_mov_b64 exec, -1\n"
    "s_barrier\n"
    "ds_read_b128 v[144:147], v232 offset:0\n"
    "ds_read_b128 v[148:151], v233 offset:0\n"
    "ds_read_b128 v[208:211], v234 offset:0\n"
    "ds_read_b128 v[212:215], v235 offset:0\n"
    "ds_read_b128 v[152:155], v232 offset:2048\n"
    "ds_read_b128 v[156:159], v233 offset:2048\n"
    "ds_read_b128 v[160:163], v232 offset:4096\n"
    "ds_read_b128 v[164:167], v233 offset:4096\n"
    "ds_read_b128 v[168:171], v232 offset:6144\n"
    "ds_read_b128 v[172:175], v233 offset:6144\n"
    "ds_read_b128 v[216:219], v234 offset:2048\n"
    "ds_read_b128 v[220:223], v235 offset:2048\n"
    "s_nop 0\n"
    ".LAL12Z_%=:\n"
    "s_waitcnt lgkmcnt(8)\n"
    "v_mfma_f32_16x16x32_bf16 v[0:3], v[144:147], v[208:211], v[0:3]\n"
    "ds_read_b128 v[224:227], v234 offset:4096\n"
    "v_mfma_f32_16x16x32_bf16 v[0:3], v[148:151], v[212:215], v[0:3]\n"
    "ds_read_b128 v[228:231], v235 offset:4096\n"
    "s_waitcnt lgkmcnt(8)\n"
    "v_mfma_f32_16x16x32_bf16 v[4:7], v[152:155], v[208:211], v[4:7]\n"
    "v_mfma_f32_16x16x32_bf16 v[4:7], v[156:159], v[212:215], v[4:7]\n"
    "s_waitcnt lgkmcnt(6)\n"
    "v_mfma_f32_16x16x32_bf16 v[8:11], v[160:163], v[208:211], v[8:11]\n"
    "s_add_u32 s81, s81, 1\n"
    "s_cmp_eq_u32 s81, 3\n"
    "v_mfma_f32_16x16x32_bf16 v[8:11], v[164:167], v[212:215], v[8:11]\n"
    "s_cselect_b32 s82, s83, 53248\n"
    "s_waitcnt lgkmcnt(4)\n"
    "v_mfma_f32_16x16x32_bf16 v[12:15], v[168:171], v[208:211], v[12:15]\n"
    "s_cselect_b32 s81, 0, s81\n"
    "v_mfma_f32_16x16x32_bf16 v[12:15], v[172:175], v[212:215], v[12:15]\n"
    "s_waitcnt lgkmcnt(2)\n"
    "v_mfma_f32_16x16x32_bf16 v[28:31], v[168:171], v[216:219], v[28:31]\n"
    "ds_read_b128 v[208:211], v234 offset:6144\n"
    "v_mfma_f32_16x16x32_bf16 v[28:31], v[172:175], v[220:223], v[28:31]\n"
    "ds_read_b128 v[212:215], v235 offset:6144\n"
    "v_mfma_f32_16x16x32_bf16 v[24:27], v[160:163], v[216:219], v[24:27]\n"
    "v_mfma_f32_16x16x32_bf16 v[24:27], v[164:167], v[220:223], v[24:27]\n"
    "v_mfma_f32_16x16x32_bf16 v[20:23], v[152:155], v[216:219], v[20:23]\n"
    "v_add_u32_e64 v232, s82, v232\n"
    "v_mfma_f32_16x16x32_bf16 v[20:23], v[156:159], v[220:223], v[20:23]\n"
    "v_add_u32_e64 v233, s82, v233\n"
    "v_mfma_f32_16x16x32_bf16 v[16:19], v[144:147], v[216:219], v[16:19]\n"
    "v_mfma_f32_16x16x32_bf16 v[16:19], v[148:151], v[220:223], v[16:19]\n"
    "s_waitcnt lgkmcnt(2)\n"
    "v_mfma_f32_16x16x32_bf16 v[32:35], v[144:147], v[224:227], v[32:35]\n"
    "ds_read_b128 v[216:219], v234 offset:8192\n"
    "v_mfma_f32_16x16x32_bf16 v[32:35], v[148:151], v[228:231], v[32:35]\n"
    "ds_read_b128 v[220:223], v235 offset:8192\n"
    "v_mfma_f32_16x16x32_bf16 v[36:39], v[152:155], v[224:227], v[36:39]\n"
    "v_mfma_f32_16x16x32_bf16 v[36:39], v[156:159], v[228:231], v[36:39]\n"
    "v_mfma_f32_16x16x32_bf16 v[40:43], v[160:163], v[224:227], v[40:43]\n"
    "v_add_u32_e64 v234, s82, v234\n"
    "v_mfma_f32_16x16x32_bf16 v[40:43], v[164:167], v[228:231], v[40:43]\n"
    "v_add_u32_e64 v235, s82, v235\n"
    "v_mfma_f32_16x16x32_bf16 v[44:47], v[168:171], v[224:227], v[44:47]\n"
    "v_mfma_f32_16x16x32_bf16 v[44:47], v[172:175], v[228:231], v[44:47]\n"
    "s_waitcnt lgkmcnt(2)\n"
    "v_mfma_f32_16x16x32_bf16 v[60:63], v[168:171], v[208:211], v[60:63]\n"
    "ds_read_b64_tr_b16 v[224:225], v236 offset:0\n"
    "v_mfma_f32_16x16x32_bf16 v[60:63], v[172:175], v[212:215], v[60:63]\n"
    "ds_read_b64_tr_b16 v[226:227], v236 offset:512\n"
    "v_mfma_f32_16x16x32_bf16 v[56:59], v[160:163], v[208:211], v[56:59]\n"
    "ds_read_b64_tr_b16 v[228:229], v236 offset:4096\n"
    "v_mfma_f32_16x16x32_bf16 v[56:59], v[164:167], v[212:215], v[56:59]\n"
    "ds_read_b64_tr_b16 v[230:231], v236 offset:4608\n"
    "v_mfma_f32_16x16x32_bf16 v[52:55], v[152:155], v[208:211], v[52:55]\n"
    "v_mfma_f32_16x16x32_bf16 v[52:55], v[156:159], v[212:215], v[52:55]\n"
    "v_mfma_f32_16x16x32_bf16 v[48:51], v[144:147], v[208:211], v[48:51]\n"
    "v_mfma_f32_16x16x32_bf16 v[48:51], v[148:151], v[212:215], v[48:51]\n"
    "s_waitcnt lgkmcnt(4)\n"
    "v_mfma_f32_16x16x32_bf16 v[64:67], v[144:147], v[216:219], v[64:67]\n"
    "ds_read_b64_tr_b16 v[208:209], v236 offset:8192\n"
    "v_mfma_f32_16x16x32_bf16 v[64:67], v[148:151], v[220:223], v[64:67]\n"
    "ds_read_b64_tr_b16 v[210:211], v236 offset:8704\n"
    "v_mfma_f32_16x16x32_bf16 v[68:71], v[152:155], v[216:219], v[68:71]\n"
    "ds_read_b64_tr_b16 v[212:213], v236 offset:12288\n"
    "v_mfma_f32_16x16x32_bf16 v[68:71], v[156:159], v[220:223], v[68:71]\n"
    "ds_read_b64_tr_b16 v[214:215], v236 offset:12800\n"
    "v_mfma_f32_16x16x32_bf16 v[72:75], v[160:163], v[216:219], v[72:75]\n"
    "v_add_u32_e64 v236, s82, v236\n"
    "v_mfma_f32_16x16x32_bf16 v[72:75], v[164:167], v[220:223], v[72:75]\n"
    "v_mfma_f32_16x16x32_bf16 v[76:79], v[168:171], v[216:219], v[76:79]\n"
    "v_mfma_f32_16x16x32_bf16 v[76:79], v[172:175], v[220:223], v[76:79]\n"
    "s_waitcnt lgkmcnt(4)\n"
    "v_mfma_f32_16x16x32_bf16 v[92:95], v[224:227], v[168:171], v[92:95]\n"
    "ds_read_b64_tr_b16 v[216:217], v237 offset:0\n"
    "v_mfma_f32_16x16x32_bf16 v[92:95], v[228:231], v[172:175], v[92:95]\n"
    "ds_read_b64_tr_b16 v[218:219], v237 offset:512\n"
    "v_mfma_f32_16x16x32_bf16 v[88:91], v[224:227], v[160:163], v[88:91]\n"
    "ds_read_b64_tr_b16 v[220:221], v237 offset:4096\n"
    "v_mfma_f32_16x16x32_bf16 v[88:91], v[228:231], v[164:167], v[88:91]\n"
    "ds_read_b64_tr_b16 v[222:223], v237 offset:4608\n"
    "v_mfma_f32_16x16x32_bf16 v[84:87], v[224:227], v[152:155], v[84:87]\n"
    "v_mfma_f32_16x16x32_bf16 v[84:87], v[228:231], v[156:159], v[84:87]\n"
    "v_mfma_f32_16x16x32_bf16 v[80:83], v[224:227], v[144:147], v[80:83]\n"
    "v_mfma_f32_16x16x32_bf16 v[80:83], v[228:231], v[148:151], v[80:83]\n"
    "s_waitcnt lgkmcnt(4)\n"
    "v_mfma_f32_16x16x32_bf16 v[96:99], v[208:211], v[144:147], v[96:99]\n"
    "ds_read_b64_tr_b16 v[224:225], v237 offset:8192\n"
    "v_mfma_f32_16x16x32_bf16 v[96:99], v[212:215], v[148:151], v[96:99]\n"
    "ds_read_b64_tr_b16 v[226:227], v237 offset:8704\n"
    "v_mfma_f32_16x16x32_bf16 v[100:103], v[208:211], v[152:155], v[100:103]\n"
    "ds_read_b64_tr_b16 v[228:229], v237 offset:12288\n"
    "v_mfma_f32_16x16x32_bf16 v[100:103], v[212:215], v[156:159], v[100:103]\n"
    "ds_read_b64_tr_b16 v[230:231], v237 offset:12800\n"
    "v_mfma_f32_16x16x32_bf16 v[104:107], v[208:211], v[160:163], v[104:107]\n"
    "v_add_u32_e64 v237, s82, v237\n"
    "v_mfma_f32_16x16x32_bf16 v[104:107], v[212:215], v[164:167], v[104:107]\n"
    "v_mfma_f32_16x16x32_bf16 v[108:111], v[208:211], v[168:171], v[108:111]\n"
    "v_mfma_f32_16x16x32_bf16 v[108:111], v[212:215], v[172:175], v[108:111]\n"
    "s_waitcnt lgkmcnt(4)\n"
    "v_mfma_f32_16x16x32_bf16 v[124:127], v[216:219], v[168:171], v[124:127]\n"
    "v_mfma_f32_16x16x32_bf16 v[124:127], v[220:223], v[172:175], v[124:127]\n"
    "s_waitcnt lgkmcnt(0)\n"
    "s_barrier\n"
    "v_mfma_f32_16x16x32_bf16 v[120:123], v[216:219], v[160:163], v[120:123]\n"
    "ds_read_b128 v[176:179], v232 offset:0\n"
    "v_mfma_f32_16x16x32_bf16 v[120:123], v[220:223], v[164:167], v[120:123]\n"
    "ds_read_b128 v[180:183], v233 offset:0\n"
    "v_mfma_f32_16x16x32_bf16 v[116:119], v[216:219], v[152:155], v[116:119]\n"
    "ds_read_b128 v[208:211], v234 offset:0\n"
    "v_mfma_f32_16x16x32_bf16 v[116:119], v[220:223], v[156:159], v[116:119]\n"
    "ds_read_b128 v[212:215], v235 offset:0\n"
    "v_mfma_f32_16x16x32_bf16 v[112:115], v[216:219], v[144:147], v[112:115]\n"
    "ds_read_b128 v[184:187], v232 offset:2048\n"
    "v_mfma_f32_16x16x32_bf16 v[112:115], v[220:223], v[148:151], v[112:115]\n"
    "ds_read_b128 v[188:191], v233 offset:2048\n"
    "v_mfma_f32_16x16x32_bf16 v[128:131], v[224:227], v[144:147], v[128:131]\n"
    "ds_read_b128 v[192:195], v232 offset:4096\n"
    "v_mfma_f32_16x16x32_bf16 v[128:131], v[228:231], v[148:151], v[128:131]\n"
    "ds_read_b128 v[196:199], v233 offset:4096\n"
    "v_mfma_f32_16x16x32_bf16 v[132:135], v[224:227], v[152:155], v[132:135]\n"
    "ds_read_b128 v[200:203], v232 offset:6144\n"
    "v_mfma_f32_16x16x32_bf16 v[132:135], v[228:231], v[156:159], v[132:135]\n"
    "ds_read_b128 v[204:207], v233 offset:6144\n"
    "v_mfma_f32_16x16x32_bf16 v[136:139], v[224:227], v[160:163], v[136:139]\n"
    "ds_read_b128 v[216:219], v234 offset:2048\n"
    "v_mfma_f32_16x16x32_bf16 v[136:139], v[228:231], v[164:167], v[136:139]\n"
    "ds_read_b128 v[220:223], v235 offset:2048\n"
    "v_mfma_f32_16x16x32_bf16 v[140:143], v[224:227], v[168:171], v[140:143]\n"
    "v_mfma_f32_16x16x32_bf16 v[140:143], v[228:231], v[172:175], v[140:143]\n"
    "s_waitcnt lgkmcnt(8)\n"
    "v_mfma_f32_16x16x32_bf16 v[0:3], v[176:179], v[208:211], v[0:3]\n"
    "ds_read_b128 v[224:227], v234 offset:4096\n"
    "v_mfma_f32_16x16x32_bf16 v[0:3], v[180:183], v[212:215], v[0:3]\n"
    "ds_read_b128 v[228:231], v235 offset:4096\n"
    "s_waitcnt lgkmcnt(8)\n"
    "v_mfma_f32_16x16x32_bf16 v[4:7], v[184:187], v[208:211], v[4:7]\n"
    "v_mfma_f32_16x16x32_bf16 v[4:7], v[188:191], v[212:215], v[4:7]\n"
    "s_waitcnt lgkmcnt(6)\n"
    "v_mfma_f32_16x16x32_bf16 v[8:11], v[192:195], v[208:211], v[8:11]\n"
    "s_add_u32 s81, s81, 1\n"
    "s_cmp_eq_u32 s81, 3\n"
    "v_mfma_f32_16x16x32_bf16 v[8:11], v[196:199], v[212:215], v[8:11]\n"
    "s_cselect_b32 s82, s83, 53248\n"
    "s_waitcnt lgkmcnt(4)\n"
    "v_mfma_f32_16x16x32_bf16 v[12:15], v[200:203], v[208:211], v[12:15]\n"
    "s_cselect_b32 s81, 0, s81\n"
    "v_mfma_f32_16x16x32_bf16 v[12:15], v[204:207], v[212:215], v[12:15]\n"
    "s_add_u32 s80, s80, 1\n"
    "s_cmp_lt_u32 s80, 3\n"
    "s_waitcnt lgkmcnt(2)\n"
    "v_mfma_f32_16x16x32_bf16 v[28:31], v[200:203], v[216:219], v[28:31]\n"
    "ds_read_b128 v[208:211], v234 offset:6144\n"
    "v_mfma_f32_16x16x32_bf16 v[28:31], v[204:207], v[220:223], v[28:31]\n"
    "ds_read_b128 v[212:215], v235 offset:6144\n"
    "v_mfma_f32_16x16x32_bf16 v[24:27], v[192:195], v[216:219], v[24:27]\n"
    "v_mfma_f32_16x16x32_bf16 v[24:27], v[196:199], v[220:223], v[24:27]\n"
    "v_mfma_f32_16x16x32_bf16 v[20:23], v[184:187], v[216:219], v[20:23]\n"
    "v_add_u32_e64 v232, s82, v232\n"
    "v_mfma_f32_16x16x32_bf16 v[20:23], v[188:191], v[220:223], v[20:23]\n"
    "v_add_u32_e64 v233, s82, v233\n"
    "v_mfma_f32_16x16x32_bf16 v[16:19], v[176:179], v[216:219], v[16:19]\n"
    "v_mfma_f32_16x16x32_bf16 v[16:19], v[180:183], v[220:223], v[16:19]\n"
    "s_waitcnt lgkmcnt(2)\n"
    "v_mfma_f32_16x16x32_bf16 v[32:35], v[176:179], v[224:227], v[32:35]\n"
    "ds_read_b128 v[216:219], v234 offset:8192\n"
    "v_mfma_f32_16x16x32_bf16 v[32:35], v[180:183], v[228:231], v[32:35]\n"
    "ds_read_b128 v[220:223], v235 offset:8192\n"
    "v_mfma_f32_16x16x32_bf16 v[36:39], v[184:187], v[224:227], v[36:39]\n"
    "v_mfma_f32_16x16x32_bf16 v[36:39], v[188:191], v[228:231], v[36:39]\n"
    "v_mfma_f32_16x16x32_bf16 v[40:43], v[192:195], v[224:227], v[40:43]\n"
    "v_add_u32_e64 v234, s82, v234\n"
    "v_mfma_f32_16x16x32_bf16 v[40:43], v[196:199], v[228:231], v[40:43]\n"
    "v_add_u32_e64 v235, s82, v235\n"
    "v_mfma_f32_16x16x32_bf16 v[44:47], v[200:203], v[224:227], v[44:47]\n"
    "v_mfma_f32_16x16x32_bf16 v[44:47], v[204:207], v[228:231], v[44:47]\n"
    "s_waitcnt lgkmcnt(2)\n"
    "v_mfma_f32_16x16x32_bf16 v[60:63], v[200:203], v[208:211], v[60:63]\n"
    "ds_read_b64_tr_b16 v[224:225], v236 offset:0\n"
    "v_mfma_f32_16x16x32_bf16 v[60:63], v[204:207], v[212:215], v[60:63]\n"
    "ds_read_b64_tr_b16 v[226:227], v236 offset:512\n"
    "v_mfma_f32_16x16x32_bf16 v[56:59], v[192:195], v[208:211], v[56:59]\n"
    "ds_read_b64_tr_b16 v[228:229], v236 offset:4096\n"
    "v_mfma_f32_16x16x32_bf16 v[56:59], v[196:199], v[212:215], v[56:59]\n"
    "ds_read_b64_tr_b16 v[230:231], v236 offset:4608\n"
    "v_mfma_f32_16x16x32_bf16 v[52:55], v[184:187], v[208:211], v[52:55]\n"
    "v_mfma_f32_16x16x32_bf16 v[52:55], v[188:191], v[212:215], v[52:55]\n"
    "v_mfma_f32_16x16x32_bf16 v[48:51], v[176:179], v[208:211], v[48:51]\n"
    "v_mfma_f32_16x16x32_bf16 v[48:51], v[180:183], v[212:215], v[48:51]\n"
    "s_waitcnt lgkmcnt(4)\n"
    "v_mfma_f32_16x16x32_bf16 v[64:67], v[176:179], v[216:219], v[64:67]\n"
    "ds_read_b64_tr_b16 v[208:209], v236 offset:8192\n"
    "v_mfma_f32_16x16x32_bf16 v[64:67], v[180:183], v[220:223], v[64:67]\n"
    "ds_read_b64_tr_b16 v[210:211], v236 offset:8704\n"
    "v_mfma_f32_16x16x32_bf16 v[68:71], v[184:187], v[216:219], v[68:71]\n"
    "ds_read_b64_tr_b16 v[212:213], v236 offset:12288\n"
    "v_mfma_f32_16x16x32_bf16 v[68:71], v[188:191], v[220:223], v[68:71]\n"
    "ds_read_b64_tr_b16 v[214:215], v236 offset:12800\n"
    "v_mfma_f32_16x16x32_bf16 v[72:75], v[192:195], v[216:219], v[72:75]\n"
    "v_add_u32_e64 v236, s82, v236\n"
    "v_mfma_f32_16x16x32_bf16 v[72:75], v[196:199], v[220:223], v[72:75]\n"
    "v_mfma_f32_16x16x32_bf16 v[76:79], v[200:203], v[216:219], v[76:79]\n"
    "v_mfma_f32_16x16x32_bf16 v[76:79], v[204:207], v[220:223], v[76:79]\n"
    "s_waitcnt lgkmcnt(4)\n"
    "v_mfma_f32_16x16x32_bf16 v[92:95], v[224:227], v[200:203], v[92:95]\n"
    "ds_read_b64_tr_b16 v[216:217], v237 offset:0\n"
    "v_mfma_f32_16x16x32_bf16 v[92:95], v[228:231], v[204:207], v[92:95]\n"
    "ds_read_b64_tr_b16 v[218:219], v237 offset:512\n"
    "v_mfma_f32_16x16x32_bf16 v[88:91], v[224:227], v[192:195], v[88:91]\n"
    "ds_read_b64_tr_b16 v[220:221], v237 offset:4096\n"
    "v_mfma_f32_16x16x32_bf16 v[88:91], v[228:231], v[196:199], v[88:91]\n"
    "ds_read_b64_tr_b16 v[222:223], v237 offset:4608\n"
    "v_mfma_f32_16x16x32_bf16 v[84:87], v[224:227], v[184:187], v[84:87]\n"
    "v_mfma_f32_16x16x32_bf16 v[84:87], v[228:231], v[188:191], v[84:87]\n"
    "v_mfma_f32_16x16x32_bf16 v[80:83], v[224:227], v[176:179], v[80:83]\n"
    "v_mfma_f32_16x16x32_bf16 v[80:83], v[228:231], v[180:183], v[80:83]\n"
    "s_waitcnt lgkmcnt(4)\n"
    "v_mfma_f32_16x16x32_bf16 v[96:99], v[208:211], v[176:179], v[96:99]\n"
    "ds_read_b64_tr_b16 v[224:225], v237 offset:8192\n"
    "v_mfma_f32_16x16x32_bf16 v[96:99], v[212:215], v[180:183], v[96:99]\n"
    "ds_read_b64_tr_b16 v[226:227], v237 offset:8704\n"
    "v_mfma_f32_16x16x32_bf16 v[100:103], v[208:211], v[184:187], v[100:103]\n"
    "ds_read_b64_tr_b16 v[228:229], v237 offset:12288\n"
    "v_mfma_f32_16x16x32_bf16 v[100:103], v[212:215], v[188:191], v[100:103]\n"
    "ds_read_b64_tr_b16 v[230:231], v237 offset:12800\n"
    "v_mfma_f32_16x16x32_bf16 v[104:107], v[208:211], v[192:195], v[104:107]\n"
    "v_add_u32_e64 v237, s82, v237\n"
    "v_mfma_f32_16x16x32_bf16 v[104:107], v[212:215], v[196:199], v[104:107]\n"
    "v_mfma_f32_16x16x32_bf16 v[108:111], v[208:211], v[200:203], v[108:111]\n"
    "v_mfma_f32_16x16x32_bf16 v[108:111], v[212:215], v[204:207], v[108:111]\n"
    "s_waitcnt lgkmcnt(4)\n"
    "v_mfma_f32_16x16x32_bf16 v[124:127], v[216:219], v[200:203], v[124:127]\n"
    "v_mfma_f32_16x16x32_bf16 v[124:127], v[220:223], v[204:207], v[124:127]\n"
    "s_waitcnt lgkmcnt(0)\n"
    "s_barrier\n"
    "v_mfma_f32_16x16x32_bf16 v[120:123], v[216:219], v[192:195], v[120:123]\n"
    "ds_read_b128 v[144:147], v232 offset:0\n"
    "v_mfma_f32_16x16x32_bf16 v[120:123], v[220:223], v[196:199], v[120:123]\n"
    "ds_read_b128 v[148:151], v233 offset:0\n"
    "v_mfma_f32_16x16x32_bf16 v[116:119], v[216:219], v[184:187], v[116:119]\n"
    "ds_read_b128 v[208:211], v234 offset:0\n"
    "v_mfma_f32_16x16x32_bf16 v[116:119], v[220:223], v[188:191], v[116:119]\n"
    "ds_read_b128 v[212:215], v235 offset:0\n"
    "v_mfma_f32_16x16x32_bf16 v[112:115], v[216:219], v[176:179], v[112:115]\n"
    "ds_read_b128 v[152:155], v232 offset:2048\n"
    "v_mfma_f32_16x16x32_bf16 v[112:115], v[220:223], v[180:183], v[112:115]\n"
    "ds_read_b128 v[156:159], v233 offset:2048\n"
    "v_mfma_f32_16x16x32_bf16 v[128:131], v[224:227], v[176:179], v[128:131]\n"
    "ds_read_b128 v[160:163], v232 offset:4096\n"
    "v_mfma_f32_16x16x32_bf16 v[128:131], v[228:231], v[180:183], v[128:131]\n"
    "ds_read_b128 v[164:167], v233 offset:4096\n"
    "v_mfma_f32_16x16x32_bf16 v[132:135], v[224:227], v[184:187], v[132:135]\n"
    "ds_read_b128 v[168:171], v232 offset:6144\n"
    "v_mfma_f32_16x16x32_bf16 v[132:135], v[228:231], v[188:191], v[132:135]\n"
    "ds_read_b128 v[172:175], v233 offset:6144\n"
    "v_mfma_f32_16x16x32_bf16 v[136:139], v[224:227], v[192:195], v[136:139]\n"
    "ds_read_b128 v[216:219], v234 offset:2048\n"
    "v_mfma_f32_16x16x32_bf16 v[136:139], v[228:231], v[196:199], v[136:139]\n"
    "ds_read_b128 v[220:223], v235 offset:2048\n"
    "v_mfma_f32_16x16x32_bf16 v[140:143], v[224:227], v[200:203], v[140:143]\n"
    "v_mfma_f32_16x16x32_bf16 v[140:143], v[228:231], v[204:207], v[140:143]\n"
    "s_cbranch_scc1 .LAL12Z_%=\n"
    "s_waitcnt lgkmcnt(8)\n"
    "v_mfma_f32_16x16x32_bf16 v[0:3], v[144:147], v[208:211], v[0:3]\n"
    "ds_read_b128 v[224:227], v234 offset:53248\n"
    "v_mfma_f32_16x16x32_bf16 v[0:3], v[148:151], v[212:215], v[0:3]\n"
    "ds_read_b128 v[228:231], v235 offset:53248\n"
    "s_waitcnt lgkmcnt(8)\n"
    "v_mfma_f32_16x16x32_bf16 v[4:7], v[152:155], v[208:211], v[4:7]\n"
    "ds_read_b128 v[176:179], v232 offset:53248\n"
    "v_mfma_f32_16x16x32_bf16 v[4:7], v[156:159], v[212:215], v[4:7]\n"
    "ds_read_b128 v[180:183], v233 offset:53248\n"
    "s_waitcnt lgkmcnt(8)\n"
    "v_mfma_f32_16x16x32_bf16 v[8:11], v[160:163], v[208:211], v[8:11]\n"
    "ds_read_b128 v[184:187], v232 offset:55296\n"
    "v_mfma_f32_16x16x32_bf16 v[8:11], v[164:167], v[212:215], v[8:11]\n"
    "ds_read_b128 v[188:191], v233 offset:55296\n"
    "s_waitcnt lgkmcnt(8)\n"
    "v_mfma_f32_16x16x32_bf16 v[12:15], v[168:171], v[208:211], v[12:15]\n"
    "ds_read_b128 v[192:195], v232 offset:57344\n"
    "v_mfma_f32_16x16x32_bf16 v[12:15], v[172:175], v[212:215], v[12:15]\n"
    "ds_read_b128 v[196:199], v233 offset:57344\n"
    "s_waitcnt lgkmcnt(8)\n"
    "v_mfma_f32_16x16x32_bf16 v[28:31], v[168:171], v[216:219], v[28:31]\n"
    "ds_read_b128 v[208:211], v234 offset:55296\n"
    "v_mfma_f32_16x16x32_bf16 v[28:31], v[172:175], v[220:223], v[28:31]\n"
    "ds_read_b128 v[212:215], v235 offset:55296\n"
    "v_mfma_f32_16x16x32_bf16 v[24:27], v[160:163], v[216:219], v[24:27]\n"
    "ds_read_b128 v[200:203], v232 offset:59392\n"
    "v_mfma_f32_16x16x32_bf16 v[24:27], v[164:167], v[220:223], v[24:27]\n"
    "ds_read_b128 v[204:207], v233 offset:59392\n"
    "v_mfma_f32_16x16x32_bf16 v[20:23], v[152:155], v[216:219], v[20:23]\n"
    "v_mfma_f32_16x16x32_bf16 v[20:23], v[156:159], v[220:223], v[20:23]\n"
    "v_mfma_f32_16x16x32_bf16 v[16:19], v[144:147], v[216:219], v[16:19]\n"
    "v_mfma_f32_16x16x32_bf16 v[16:19], v[148:151], v[220:223], v[16:19]\n"
    "s_waitcnt lgkmcnt(8)\n"
    "v_mfma_f32_16x16x32_bf16 v[0:3], v[176:179], v[224:227], v[0:3]\n"
    "ds_read_b128 v[216:219], v234 offset:4096\n"
    "v_mfma_f32_16x16x32_bf16 v[0:3], v[180:183], v[228:231], v[0:3]\n"
    "ds_read_b128 v[220:223], v235 offset:4096\n"
    "s_waitcnt lgkmcnt(8)\n"
    "v_mfma_f32_16x16x32_bf16 v[4:7], v[184:187], v[224:227], v[4:7]\n"
    "v_mfma_f32_16x16x32_bf16 v[4:7], v[188:191], v[228:231], v[4:7]\n"
    "s_waitcnt lgkmcnt(6)\n"
    "v_mfma_f32_16x16x32_bf16 v[8:11], v[192:195], v[224:227], v[8:11]\n"
    "v_mfma_f32_16x16x32_bf16 v[8:11], v[196:199], v[228:231], v[8:11]\n"
    "s_waitcnt lgkmcnt(2)\n"
    "v_mfma_f32_16x16x32_bf16 v[12:15], v[200:203], v[224:227], v[12:15]\n"
    "v_mfma_f32_16x16x32_bf16 v[12:15], v[204:207], v[228:231], v[12:15]\n"
    "s_nop 0\n"
    "s_barrier\n"
    "v_mfma_f32_16x16x32_bf16 v[28:31], v[200:203], v[208:211], v[28:31]\n"
    "ds_read_b128 v[224:227], v234 offset:57344\n"
    "v_mfma_f32_16x16x32_bf16 v[28:31], v[204:207], v[212:215], v[28:31]\n"
    "ds_read_b128 v[228:231], v235 offset:57344\n"
    "v_mfma_f32_16x16x32_bf16 v[24:27], v[192:195], v[208:211], v[24:27]\n"
    "ds_read_b32 v248, v254 offset:0\n"
    "v_mfma_f32_16x16x32_bf16 v[24:27], v[196:199], v[212:215], v[24:27]\n"
    "ds_read_b32 v249, v254 offset:64\n"
    "v_mfma_f32_16x16x32_bf16 v[20:23], v[184:187], v[208:211], v[20:23]\n"
    "ds_read_b32 v250, v254 offset:128\n"
    "v_cvt_pk_bf16_f32 v0, v0, v1\n"
    "v_mfma_f32_16x16x32_bf16 v[20:23], v[188:191], v[212:215], v[20:23]\n"
    "ds_read_b32 v251, v254 offset:192\n"
    "v_cvt_pk_bf16_f32 v1, v2, v3\n"
    "v_mfma_f32_16x16x32_bf16 v[16:19], v[176:179], v[208:211], v[16:19]\n"
    "ds_read2st64_b32 v[252:253], v254 offset0:4 offset1:6\n"
    "v_cvt_pk_bf16_f32 v2, v4, v5\n"
    "v_mfma_f32_16x16x32_bf16 v[16:19], v[180:183], v[212:215], v[16:19]\n"
    "v_cvt_pk_bf16_f32 v3, v6, v7\n"
    "buffer_store_dwordx4 v[0:3], v247, s[60:63], s93 offen offset:0 sc1\n"
    "s_waitcnt lgkmcnt(7)\n"
    "v_mfma_f32_16x16x32_bf16 v[32:35], v[144:147], v[216:219], v[32:35]\n"
    "ds_read_b128 v[208:211], v234 offset:6144\n"
    "v_cvt_pk_bf16_f32 v8, v8, v9\n"
    "v_mfma_f32_16x16x32_bf16 v[32:35], v[148:151], v[220:223], v[32:35]\n"
    "ds_read_b128 v[212:215], v235 offset:6144\n"
    "v_cvt_pk_bf16_f32 v9, v10, v11\n"
    "v_mfma_f32_16x16x32_bf16 v[36:39], v[152:155], v[216:219], v[36:39]\n"
    "v_cvt_pk_bf16_f32 v10, v12, v13\n"
    "v_mfma_f32_16x16x32_bf16 v[36:39], v[156:159], v[220:223], v[36:39]\n"
    "v_cvt_pk_bf16_f32 v11, v14, v15\n"
    "buffer_store_dwordx4 v[8:11], v247, s[60:63], s93 offen offset:1024 sc1\n"
    "v_mfma_f32_16x16x32_bf16 v[40:43], v[160:163], v[216:219], v[40:43]\n"
    "v_cvt_pk_bf16_f32 v24, v24, v25\n"
    "v_mfma_f32_16x16x32_bf16 v[40:43], v[164:167], v[220:223], v[40:43]\n"
    "v_cvt_pk_bf16_f32 v25, v26, v27\n"
    "v_mfma_f32_16x16x32_bf16 v[44:47], v[168:171], v[216:219], v[44:47]\n"
    "v_cvt_pk_bf16_f32 v26, v28, v29\n"
    "v_mfma_f32_16x16x32_bf16 v[44:47], v[172:175], v[220:223], v[44:47]\n"
    "v_cvt_pk_bf16_f32 v27, v30, v31\n"
    "buffer_store_dwordx4 v[24:27], v247, s[60:63], s93 offen offset:3072 sc1\n"
    "s_waitcnt lgkmcnt(7)\n"
    "v_mfma_f32_16x16x32_bf16 v[44:47], v[200:203], v[224:227], v[44:47]\n"
    "ds_read_b128 v[216:219], v234 offset:59392\n"
    "v_cvt_pk_bf16_f32 v16, v16, v17\n"
    "v_mfma_f32_16x16x32_bf16 v[44:47], v[204:207], v[228:231], v[44:47]\n"
    "ds_read_b128 v[220:223], v235 offset:59392\n"
    "v_cvt_pk_bf16_f32 v17, v18, v19\n"
    "v_mfma_f32_16x16x32_bf16 v[40:43], v[192:195], v[224:227], v[40:43]\n"
    "v_cvt_pk_bf16_f32 v18, v20, v21\n"
    "v_mfma_f32_16x16x32_bf16 v[40:43], v[196:199], v[228:231], v[40:43]\n"
    "v_cvt_pk_bf16_f32 v19, v22, v23\n"
    "buffer_store_dwordx4 v[16:19], v247, s[60:63], s93 offen offset:2048 sc1\n"
    "v_mfma_f32_16x16x32_bf16 v[36:39], v[184:187], v[224:227], v[36:39]\n"
    "v_mfma_f32_16x16x32_bf16 v[36:39], v[188:191], v[228:231], v[36:39]\n"
    "v_mfma_f32_16x16x32_bf16 v[32:35], v[176:179], v[224:227], v[32:35]\n"
    "v_mfma_f32_16x16x32_bf16 v[32:35], v[180:183], v[228:231], v[32:35]\n"
    "s_waitcnt lgkmcnt(2)\n"
    "v_mfma_f32_16x16x32_bf16 v[48:51], v[144:147], v[208:211], v[48:51]\n"
    "ds_read_b128 v[224:227], v234 offset:8192\n"
    "v_mfma_f32_16x16x32_bf16 v[48:51], v[148:151], v[212:215], v[48:51]\n"
    "ds_read_b128 v[228:231], v235 offset:8192\n"
    "v_mfma_f32_16x16x32_bf16 v[52:55], v[152:155], v[208:211], v[52:55]\n"
    "v_mfma_f32_16x16x32_bf16 v[52:55], v[156:159], v[212:215], v[52:55]\n"
    "v_mfma_f32_16x16x32_bf16 v[56:59], v[160:163], v[208:211], v[56:59]\n"
    "v_cvt_pk_bf16_f32 v40, v40, v41\n"
    "v_mfma_f32_16x16x32_bf16 v[56:59], v[164:167], v[212:215], v[56:59]\n"
    "v_cvt_pk_bf16_f32 v41, v42, v43\n"
    "v_mfma_f32_16x16x32_bf16 v[60:63], v[168:171], v[208:211], v[60:63]\n"
    "v_cvt_pk_bf16_f32 v42, v44, v45\n"
    "v_mfma_f32_16x16x32_bf16 v[60:63], v[172:175], v[212:215], v[60:63]\n"
    "v_cvt_pk_bf16_f32 v43, v46, v47\n"
    "buffer_store_dwordx4 v[40:43], v247, s[60:63], s94 offen offset:1024 sc1\n"
    "s_waitcnt lgkmcnt(2)\n"
    "v_mfma_f32_16x16x32_bf16 v[60:63], v[200:203], v[216:219], v[60:63]\n"
    "ds_read_b128 v[208:211], v234 offset:61440\n"
    "v_cvt_pk_bf16_f32 v32, v32, v33\n"
    "v_mfma_f32_16x16x32_bf16 v[60:63], v[204:207], v[220:223], v[60:63]\n"
    "ds_read_b128 v[212:215], v235 offset:61440\n"
    "v_cvt_pk_bf16_f32 v33, v34, v35\n"
    "v_mfma_f32_16x16x32_bf16 v[56:59], v[192:195], v[216:219], v[56:59]\n"
    "v_cvt_pk_bf16_f32 v34, v36, v37\n"
    "v_mfma_f32_16x16x32_bf16 v[56:59], v[196:199], v[220:223], v[56:59]\n"
    "v_cvt_pk_bf16_f32 v35, v38, v39\n"
    "buffer_store_dwordx4 v[32:35], v247, s[60:63], s94 offen offset:0 sc1\n"
    "v_mfma_f32_16x16x32_bf16 v[52:55], v[184:187], v[216:219], v[52:55]\n"
    "v_mfma_f32_16x16x32_bf16 v[52:55], v[188:191], v[220:223], v[52:55]\n"
    "v_mfma_f32_16x16x32_bf16 v[48:51], v[176:179], v[216:219], v[48:51]\n"
    "v_mfma_f32_16x16x32_bf16 v[48:51], v[180:183], v[220:223], v[48:51]\n"
    "s_waitcnt lgkmcnt(2)\n"
    "v_mfma_f32_16x16x32_bf16 v[64:67], v[144:147], v[224:227], v[64:67]\n"
    "ds_read_b64_tr_b16 v[216:217], v236 offset:0\n"
    "v_mfma_f32_16x16x32_bf16 v[64:67], v[148:151], v[228:231], v[64:67]\n"
    "ds_read_b64_tr_b16 v[218:219], v236 offset:512\n"
    "v_mfma_f32_16x16x32_bf16 v[68:71], v[152:155], v[224:227], v[68:71]\n"
    "ds_read_b64_tr_b16 v[220:221], v236 offset:4096\n"
    "v_mfma_f32_16x16x32_bf16 v[68:71], v[156:159], v[228:231], v[68:71]\n"
    "ds_read_b64_tr_b16 v[222:223], v236 offset:4608\n"
    "v_mfma_f32_16x16x32_bf16 v[72:75], v[160:163], v[224:227], v[72:75]\n"
    "v_cvt_pk_bf16_f32 v56, v56, v57\n"
    "v_mfma_f32_16x16x32_bf16 v[72:75], v[164:167], v[228:231], v[72:75]\n"
    "v_cvt_pk_bf16_f32 v57, v58, v59\n"
    "v_mfma_f32_16x16x32_bf16 v[76:79], v[168:171], v[224:227], v[76:79]\n"
    "v_cvt_pk_bf16_f32 v58, v60, v61\n"
    "v_mfma_f32_16x16x32_bf16 v[76:79], v[172:175], v[228:231], v[76:79]\n"
    "v_cvt_pk_bf16_f32 v59, v62, v63\n"
    "buffer_store_dwordx4 v[56:59], v247, s[60:63], s94 offen offset:3072 sc1\n"
    "s_waitcnt lgkmcnt(4)\n"
    "v_mfma_f32_16x16x32_bf16 v[76:79], v[200:203], v[208:211], v[76:79]\n"
    "ds_read_b64_tr_b16 v[224:225], v236 offset:8192\n"
    ".LPAD4_%=:\n"
    "s_cbranch_execz .LRET_%=\n"
    "s_nop 0\n"
    "v_cvt_pk_bf16_f32 v48, v48, v49\n"
    "v_mfma_f32_16x16x32_bf16 v[76:79], v[204:207], v[212:215], v[76:79]\n"
    "ds_read_b64_tr_b16 v[226:227], v236 offset:8704\n"
    "v_cvt_pk_bf16_f32 v49, v50, v51\n"
    "v_mfma_f32_16x16x32_bf16 v[72:75], v[192:195], v[208:211], v[72:75]\n"
    "ds_read_b64_tr_b16 v[228:229], v236 offset:12288\n"
    "v_cvt_pk_bf16_f32 v50, v52, v53\n"
    "v_mfma_f32_16x16x32_bf16 v[72:75], v[196:199], v[212:215], v[72:75]\n"
    "ds_read_b64_tr_b16 v[230:231], v236 offset:12800\n"
    "v_cvt_pk_bf16_f32 v51, v54, v55\n"
    "buffer_store_dwordx4 v[48:51], v247, s[60:63], s94 offen offset:2048 sc1\n"
    "v_mfma_f32_16x16x32_bf16 v[68:71], v[184:187], v[208:211], v[68:71]\n"
    "v_mfma_f32_16x16x32_bf16 v[68:71], v[188:191], v[212:215], v[68:71]\n"
    "v_mfma_f32_16x16x32_bf16 v[64:67], v[176:179], v[208:211], v[64:67]\n"
    "v_mfma_f32_16x16x32_bf16 v[64:67], v[180:183], v[212:215], v[64:67]\n"
    "v_add_u32 v236, 53248, v236\n"
    "s_waitcnt lgkmcnt(4)\n"
    "v_mfma_f32_16x16x32_bf16 v[80:83], v[216:219], v[144:147], v[80:83]\n"
    "ds_read_b64_tr_b16 v[208:209], v236 offset:0\n"
    "v_mfma_f32_16x16x32_bf16 v[80:83], v[220:223], v[148:151], v[80:83]\n"
    "ds_read_b64_tr_b16 v[210:211], v236 offset:512\n"
    "v_mfma_f32_16x16x32_bf16 v[84:87], v[216:219], v[152:155], v[84:87]\n"
    "ds_read_b64_tr_b16 v[212:213], v236 offset:4096\n"
    "v_mfma_f32_16x16x32_bf16 v[84:87], v[220:223], v[156:159], v[84:87]\n"
    "ds_read_b64_tr_b16 v[214:215], v236 offset:4608\n"
    "v_mfma_f32_16x16x32_bf16 v[88:91], v[216:219], v[160:163], v[88:91]\n"
    "v_cvt_pk_bf16_f32 v72, v72, v73\n"
    "v_mfma_f32_16x16x32_bf16 v[88:91], v[220:223], v[164:167], v[88:91]\n"
    "v_cvt_pk_bf16_f32 v73, v74, v75\n"
    "v_mfma_f32_16x16x32_bf16 v[92:95], v[216:219], v[168:171], v[92:95]\n"
    "v_cvt_pk_bf16_f32 v74, v76, v77\n"
    "v_mfma_f32_16x16x32_bf16 v[92:95], v[220:223], v[172:175], v[92:95]\n"
    "v_cvt_pk_bf16_f32 v75, v78, v79\n"
    "buffer_store_dwordx4 v[72:75], v247, s[60:63], s95 offen offset:1024 sc1\n"
    "s_waitcnt lgkmcnt(4)\n"
    "v_mfma_f32_16x16x32_bf16 v[108:111], v[224:227], v[168:171], v[108:111]\n"
    "ds_read_b64_tr_b16 v[216:217], v236 offset:8192\n"
    "v_cvt_pk_bf16_f32 v64, v64, v65\n"
    "v_mfma_f32_16x16x32_bf16 v[108:111], v[228:231], v[172:175], v[108:111]\n"
    "ds_read_b64_tr_b16 v[218:219], v236 offset:8704\n"
    "v_cvt_pk_bf16_f32 v65, v66, v67\n"
    "v_mfma_f32_16x16x32_bf16 v[104:107], v[224:227], v[160:163], v[104:107]\n"
    "ds_read_b64_tr_b16 v[220:221], v236 offset:12288\n"
    "v_cvt_pk_bf16_f32 v66, v68, v69\n"
    "v_mfma_f32_16x16x32_bf16 v[104:107], v[228:231], v[164:167], v[104:107]\n"
    "ds_read_b64_tr_b16 v[222:223], v236 offset:12800\n"
    "v_cvt_pk_bf16_f32 v67, v70, v71\n"
    "buffer_store_dwordx4 v[64:67], v247, s[60:63], s95 offen offset:0 sc1\n"
    "v_mfma_f32_16x16x32_bf16 v[100:103], v[224:227], v[152:155], v[100:103]\n"
    "s_nop 0\n"
    "s_barrier\n"
    "v_mfma_f32_16x16x32_bf16 v[100:103], v[228:231], v[156:159], v[100:103]\n"
    "v_mfma_f32_16x16x32_bf16 v[96:99], v[224:227], v[144:147], v[96:99]\n"
    "v_mfma_f32_16x16x32_bf16 v[96:99], v[228:231], v[148:151], v[96:99]\n"
    "s_waitcnt lgkmcnt(4)\n"
    "v_mfma_f32_16x16x32_bf16 v[80:83], v[208:211], v[176:179], v[80:83]\n"
    "ds_read_b64_tr_b16 v[224:225], v237 offset:0\n"
    "v_mfma_f32_16x16x32_bf16 v[80:83], v[212:215], v[180:183], v[80:83]\n"
    "ds_read_b64_tr_b16 v[226:227], v237 offset:512\n"
    "v_mfma_f32_16x16x32_bf16 v[84:87], v[208:211], v[184:187], v[84:87]\n"
    "ds_read_b64_tr_b16 v[228:229], v237 offset:4096\n"
    "v_mfma_f32_16x16x32_bf16 v[84:87], v[212:215], v[188:191], v[84:87]\n"
    "ds_read_b64_tr_b16 v[230:231], v237 offset:4608\n"
    "v_mfma_f32_16x16x32_bf16 v[88:91], v[208:211], v[192:195], v[88:91]\n"
    "v_mfma_f32_16x16x32_bf16 v[88:91], v[212:215], v[196:199], v[88:91]\n"
    "v_mfma_f32_16x16x32_bf16 v[92:95], v[208:211], v[200:203], v[92:95]\n"
    "v_mfma_f32_16x16x32_bf16 v[92:95], v[212:215], v[204:207], v[92:95]\n"
    "s_waitcnt lgkmcnt(4)\n"
    "v_mfma_f32_16x16x32_bf16 v[108:111], v[216:219], v[200:203], v[108:111]\n"
    "ds_read_b64_tr_b16 v[208:209], v237 offset:8192\n"
    "v_mfma_f32_16x16x32_bf16 v[108:111], v[220:223], v[204:207], v[108:111]\n"
    "ds_read_b64_tr_b16 v[210:211], v237 offset:8704\n"
    "v_mfma_f32_16x16x32_bf16 v[104:107], v[216:219], v[192:195], v[104:107]\n"
    "ds_read_b64_tr_b16 v[212:213], v237 offset:12288\n"
    "v_mfma_f32_16x16x32_bf16 v[104:107], v[220:223], v[196:199], v[104:107]\n"
    "ds_read_b64_tr_b16 v[214:215], v237 offset:12800\n"
    "v_mfma_f32_16x16x32_bf16 v[100:103], v[216:219], v[184:187], v[100:103]\n"
    "v_mfma_f32_16x16x32_bf16 v[100:103], v[220:223], v[188:191], v[100:103]\n"
    "v_mfma_f32_16x16x32_bf16 v[96:99], v[216:219], v[176:179], v[96:99]\n"
    "v_mfma_f32_16x16x32_bf16 v[96:99], v[220:223], v[180:183], v[96:99]\n"
    "v_add_u32 v237, 53248, v237\n"
    "s_waitcnt lgkmcnt(4)\n"
    "v_mfma_f32_16x16x32_bf16 v[112:115], v[224:227], v[144:147], v[112:115]\n"
    "ds_read_b64_tr_b16 v[216:217], v237 offset:0\n"
    "v_mfma_f32_16x16x32_bf16 v[112:115], v[228:231], v[148:151], v[112:115]\n"
    "ds_read_b64_tr_b16 v[218:219], v237 offset:512\n"
    "v_mfma_f32_16x16x32_bf16 v[116:119], v[224:227], v[152:155], v[116:119]\n"
    "ds_read_b64_tr_b16 v[220:221], v237 offset:4096\n"
    "v_mfma_f32_16x16x32_bf16 v[116:119], v[228:231], v[156:159], v[116:119]\n"
    "ds_read_b64_tr_b16 v[222:223], v237 offset:4608\n"
    "v_mfma_f32_16x16x32_bf16 v[120:123], v[224:227], v[160:163], v[120:123]\n"
    "v_mfma_f32_16x16x32_bf16 v[120:123], v[228:231], v[164:167], v[120:123]\n"
    "v_mfma_f32_16x16x32_bf16 v[124:127], v[224:227], v[168:171], v[124:127]\n"
    "v_mfma_f32_16x16x32_bf16 v[124:127], v[228:231], v[172:175], v[124:127]\n"
    "v_mul_f32 v108, v108, v92\n"
    "v_mul_f32 v109, v109, v93\n"
    "s_waitcnt lgkmcnt(4)\n"
    "v_mfma_f32_16x16x32_bf16 v[140:143], v[208:211], v[168:171], v[140:143]\n"
    "ds_read_b64_tr_b16 v[224:225], v237 offset:8192\n"
    "v_mul_f32 v110, v110, v94\n"
    "v_mfma_f32_16x16x32_bf16 v[140:143], v[212:215], v[172:175], v[140:143]\n"
    "ds_read_b64_tr_b16 v[226:227], v237 offset:8704\n"
    "v_mul_f32 v111, v111, v95\n"
    "v_mfma_f32_16x16x32_bf16 v[136:139], v[208:211], v[160:163], v[136:139]\n"
    "ds_read_b64_tr_b16 v[228:229], v237 offset:12288\n"
    "v_mul_f32 v92, s84, v92\n"
    "v_mfma_f32_16x16x32_bf16 v[136:139], v[212:215], v[164:167], v[136:139]\n"
    "ds_read_b64_tr_b16 v[230:231], v237 offset:12800\n"
    "v_mul_f32 v93, s84, v93\n"
    "v_mfma_f32_16x16x32_bf16 v[132:135], v[208:211], v[152:155], v[132:135]\n"
    "v_mul_f32 v94, s84, v94\n"
    "v_mul_f32 v95, s84, v95\n"
    "v_mfma_f32_16x16x32_bf16 v[132:135], v[212:215], v[156:159], v[132:135]\n"
    "v_exp_f32 v92, v92\n"
    "v_mfma_f32_16x16x32_bf16 v[128:131], v[208:211], v[144:147], v[128:131]\n"
    "v_exp_f32 v93, v93\n"
    "v_mfma_f32_16x16x32_bf16 v[128:131], v[212:215], v[148:151], v[128:131]\n"
    "v_exp_f32 v94, v94\n"
    "s_cmp_eq_u32 s74, 0\n"
    "s_waitcnt lgkmcnt(4)\n"
    "v_mfma_f32_16x16x32_bf16 v[112:115], v[216:219], v[176:179], v[112:115]\n"
    "v_exp_f32 v95, v95\n"
    "s_waitcnt vmcnt(0) lgkmcnt(0)\n"
    "s_barrier\n"
    "s_cbranch_scc0 .LAF6_%=\n"
    "s_mov_b32 exec_lo, 4294967295\n"
    "s_mov_b32 exec_hi, 0\n"
    "buffer_store_dwordx2 v[252:253], v243, s[60:63], 0 offen sc1\n"
    "s_mov_b64 exec, -1\n"
    ".LAF6_%=:\n"
    "v_mfma_f32_16x16x32_bf16 v[112:115], v[220:223], v[180:183], v[112:115]\n"
    "v_fma_f32 v92, v92, v251, v251\n"
    "v_fma_f32 v93, v93, v251, v251\n"
    "v_mfma_f32_16x16x32_bf16 v[116:119], v[216:219], v[184:187], v[116:119]\n"
    "v_fma_f32 v94, v94, v251, v251\n"
    "v_fma_f32 v95, v95, v251, v251\n"
    "v_mfma_f32_16x16x32_bf16 v[116:119], v[220:223], v[188:191], v[116:119]\n"
    "v_rcp_f32 v92, v92\n"
    "v_mfma_f32_16x16x32_bf16 v[120:123], v[216:219], v[192:195], v[120:123]\n"
    "v_rcp_f32 v93, v93\n"
    "v_mfma_f32_16x16x32_bf16 v[120:123], v[220:223], v[196:199], v[120:123]\n"
    "v_rcp_f32 v94, v94\n"
    "v_mfma_f32_16x16x32_bf16 v[124:127], v[216:219], v[200:203], v[124:127]\n"
    "v_rcp_f32 v95, v95\n"
    "v_mfma_f32_16x16x32_bf16 v[124:127], v[220:223], v[204:207], v[124:127]\n"
    "v_mul_f32 v108, v108, v92\n"
    "v_mul_f32 v109, v109, v93\n"
    "v_mfma_f32_16x16x32_bf16 v[140:143], v[224:227], v[200:203], v[140:143]\n"
    "v_mul_f32 v110, v110, v94\n"
    "v_mul_f32 v111, v111, v95\n"
    "v_mfma_f32_16x16x32_bf16 v[140:143], v[228:231], v[204:207], v[140:143]\n"
    "v_cvt_pk_bf16_f32 v12, v108, v109\n"
    "v_cvt_pk_bf16_f32 v13, v110, v111\n"
    "v_mfma_f32_16x16x32_bf16 v[136:139], v[224:227], v[192:195], v[136:139]\n"
    "v_mul_f32 v104, v104, v88\n"
    "v_mul_f32 v105, v105, v89\n"
    "v_mfma_f32_16x16x32_bf16 v[136:139], v[228:231], v[196:199], v[136:139]\n"
    "v_mul_f32 v106, v106, v90\n"
    "v_mul_f32 v107, v107, v91\n"
    "v_mfma_f32_16x16x32_bf16 v[132:135], v[224:227], v[184:187], v[132:135]\n"
    "v_mul_f32 v88, s84, v88\n"
    "v_mul_f32 v89, s84, v89\n"
    "v_mfma_f32_16x16x32_bf16 v[132:135], v[228:231], v[188:191], v[132:135]\n"
    "v_mul_f32 v90, s84, v90\n"
    "v_mul_f32 v91, s84, v91\n"
    "v_mfma_f32_16x16x32_bf16 v[128:131], v[224:227], v[176:179], v[128:131]\n"
    "v_exp_f32 v88, v88\n"
    "v_mfma_f32_16x16x32_bf16 v[128:131], v[228:231], v[180:183], v[128:131]\n"
    "v_exp_f32 v89, v89\n"
    "s_waitcnt lgkmcnt(0)\n"
    "v_exp_f32 v90, v90\n"
    "v_exp_f32 v91, v91\n"
    "v_fma_f32 v88, v88, v250, v250\n"
    "v_fma_f32 v89, v89, v250, v250\n"
    "v_fma_f32 v90, v90, v250, v250\n"
    "v_fma_f32 v91, v91, v250, v250\n"
    "v_rcp_f32 v88, v88\n"
    "v_rcp_f32 v89, v89\n"
    "v_rcp_f32 v90, v90\n"
    "v_rcp_f32 v91, v91\n"
    "v_mul_f32 v104, v104, v88\n"
    "v_mul_f32 v105, v105, v89\n"
    "v_mul_f32 v106, v106, v90\n"
    "v_mul_f32 v107, v107, v91\n"
    "v_cvt_pk_bf16_f32 v8, v104, v105\n"
    "v_cvt_pk_bf16_f32 v9, v106, v107\n"
    "s_nop 7\n"
    "v_pk_mul_f32 v[100:101], v[100:101], v[84:85]\n"
    "v_pk_mul_f32 v[96:97], v[96:97], v[80:81]\n"
    "v_pk_mul_f32 v[102:103], v[102:103], v[86:87]\n"
    "v_pk_mul_f32 v[98:99], v[98:99], v[82:83]\n"
    "v_pk_mul_f32 v[84:85], v[84:85], s[84:85]\n"
    "v_pk_mul_f32 v[80:81], v[80:81], s[84:85]\n"
    "v_pk_mul_f32 v[86:87], v[86:87], s[84:85]\n"
    "v_pk_mul_f32 v[82:83], v[82:83], s[84:85]\n"
    "v_exp_f32 v84, v84\n"
    "v_exp_f32 v80, v80\n"
    "v_exp_f32 v85, v85\n"
    "v_exp_f32 v81, v81\n"
    "v_exp_f32 v86, v86\n"
    "v_exp_f32 v82, v82\n"
    "v_exp_f32 v87, v87\n"
    "v_exp_f32 v83, v83\n"
    "v_pk_fma_f32 v[84:85], v[84:85], v[248:249], v[248:249] op_sel:[0,1,1] op_sel_hi:[1,1,1]\n"
    "v_pk_fma_f32 v[80:81], v[80:81], v[248:249], v[248:249] op_sel:[0,0,0] op_sel_hi:[1,0,0]\n"
    "v_pk_fma_f32 v[86:87], v[86:87], v[248:249], v[248:249] op_sel:[0,1,1] op_sel_hi:[1,1,1]\n"
    "v_pk_fma_f32 v[82:83], v[82:83], v[248:249], v[248:249] op_sel:[0,0,0] op_sel_hi:[1,0,0]\n"
    "v_rcp_f32 v84, v84\n"
    "v_rcp_f32 v80, v80\n"
    "v_rcp_f32 v85, v85\n"
    "v_rcp_f32 v81, v81\n"
    "v_rcp_f32 v86, v86\n"
    "v_rcp_f32 v82, v82\n"
    "v_rcp_f32 v87, v87\n"
    "v_rcp_f32 v83, v83\n"
    "v_pk_mul_f32 v[100:101], v[100:101], v[84:85]\n"
    "v_pk_mul_f32 v[96:97], v[96:97], v[80:81]\n"
    "v_pk_mul_f32 v[102:103], v[102:103], v[86:87]\n"
    "v_pk_mul_f32 v[98:99], v[98:99], v[82:83]\n"
    "v_cvt_pk_bf16_f32 v4, v100, v101\n"
    "v_cvt_pk_bf16_f32 v0, v96, v97\n"
    "v_cvt_pk_bf16_f32 v5, v102, v103\n"
    "v_cvt_pk_bf16_f32 v1, v98, v99\n"
    "v_pk_mul_f32 v[140:141], v[140:141], v[124:125]\n"
    "v_pk_mul_f32 v[136:137], v[136:137], v[120:121]\n"
    "v_pk_mul_f32 v[142:143], v[142:143], v[126:127]\n"
    "v_pk_mul_f32 v[138:139], v[138:139], v[122:123]\n"
    "v_pk_mul_f32 v[124:125], v[124:125], s[84:85]\n"
    "v_pk_mul_f32 v[120:121], v[120:121], s[84:85]\n"
    "v_pk_mul_f32 v[126:127], v[126:127], s[84:85]\n"
    "v_pk_mul_f32 v[122:123], v[122:123], s[84:85]\n"
    "v_exp_f32 v124, v124\n"
    "v_exp_f32 v120, v120\n"
    "v_exp_f32 v125, v125\n"
    "v_exp_f32 v121, v121\n"
    "v_exp_f32 v126, v126\n"
    "v_exp_f32 v122, v122\n"
    "v_exp_f32 v127, v127\n"
    "v_exp_f32 v123, v123\n"
    "v_pk_fma_f32 v[124:125], v[124:125], v[250:251], v[250:251] op_sel:[0,1,1] op_sel_hi:[1,1,1]\n"
    "v_pk_fma_f32 v[120:121], v[120:121], v[250:251], v[250:251] op_sel:[0,0,0] op_sel_hi:[1,0,0]\n"
    "v_pk_fma_f32 v[126:127], v[126:127], v[250:251], v[250:251] op_sel:[0,1,1] op_sel_hi:[1,1,1]\n"
    "v_pk_fma_f32 v[122:123], v[122:123], v[250:251], v[250:251] op_sel:[0,0,0] op_sel_hi:[1,0,0]\n"
    "v_rcp_f32 v124, v124\n"
    "v_rcp_f32 v120, v120\n"
    "v_rcp_f32 v125, v125\n"
    "v_rcp_f32 v121, v121\n"
    "v_rcp_f32 v126, v126\n"
    "v_rcp_f32 v122, v122\n"
    "v_rcp_f32 v127, v127\n"
    "v_rcp_f32 v123, v123\n"
    "v_pk_mul_f32 v[140:141], v[140:141], v[124:125]\n"
    "v_pk_mul_f32 v[136:137], v[136:137], v[120:121]\n"
    "v_pk_mul_f32 v[142:143], v[142:143], v[126:127]\n"
    "v_pk_mul_f32 v[138:139], v[138:139], v[122:123]\n"
    "v_cvt_pk_bf16_f32 v14, v140, v141\n"
    "v_cvt_pk_bf16_f32 v10, v136, v137\n"
    "v_cvt_pk_bf16_f32 v15, v142, v143\n"
    "v_cvt_pk_bf16_f32 v11, v138, v139\n"
    "v_pk_mul_f32 v[132:133], v[132:133], v[116:117]\n"
    "v_pk_mul_f32 v[128:129], v[128:129], v[112:113]\n"
    "v_pk_mul_f32 v[134:135], v[134:135], v[118:119]\n"
    "v_pk_mul_f32 v[130:131], v[130:131], v[114:115]\n"
    "v_pk_mul_f32 v[116:117], v[116:117], s[84:85]\n"
    "v_pk_mul_f32 v[112:113], v[112:113], s[84:85]\n"
    "v_pk_mul_f32 v[118:119], v[118:119], s[84:85]\n"
    "v_pk_mul_f32 v[114:115], v[114:115], s[84:85]\n"
    "v_exp_f32 v116, v116\n"
    "v_exp_f32 v112, v112\n"
    "v_exp_f32 v117, v117\n"
    "v_exp_f32 v113, v113\n"
    "v_exp_f32 v118, v118\n"
    "v_exp_f32 v114, v114\n"
    "v_exp_f32 v119, v119\n"
    "v_exp_f32 v115, v115\n"
    "v_pk_fma_f32 v[116:117], v[116:117], v[248:249], v[248:249] op_sel:[0,1,1] op_sel_hi:[1,1,1]\n"
    "v_pk_fma_f32 v[112:113], v[112:113], v[248:249], v[248:249] op_sel:[0,0,0] op_sel_hi:[1,0,0]\n"
    "v_pk_fma_f32 v[118:119], v[118:119], v[248:249], v[248:249] op_sel:[0,1,1] op_sel_hi:[1,1,1]\n"
    "v_pk_fma_f32 v[114:115], v[114:115], v[248:249], v[248:249] op_sel:[0,0,0] op_sel_hi:[1,0,0]\n"
    "v_rcp_f32 v116, v116\n"
    "v_rcp_f32 v112, v112\n"
    "v_rcp_f32 v117, v117\n"
    "v_rcp_f32 v113, v113\n"
    "v_rcp_f32 v118, v118\n"
    "v_rcp_f32 v114, v114\n"
    "v_rcp_f32 v119, v119\n"
    "v_rcp_f32 v115, v115\n"
    "v_pk_mul_f32 v[132:133], v[132:133], v[116:117]\n"
    "v_pk_mul_f32 v[128:129], v[128:129], v[112:113]\n"
    "v_pk_mul_f32 v[134:135], v[134:135], v[118:119]\n"
    "v_pk_mul_f32 v[130:131], v[130:131], v[114:115]\n"
    "v_cvt_pk_bf16_f32 v6, v132, v133\n"
    "v_cvt_pk_bf16_f32 v2, v128, v129\n"
    "v_cvt_pk_bf16_f32 v7, v134, v135\n"
    "v_cvt_pk_bf16_f32 v3, v130, v131\n"
    ".LA3_%=:\n"
    ".LPAD5_%=:\n"
    "s_cbranch_execz .LRET_%=\n"
    "s_nop 0\n"
    "s_setprio 3\n"
    "s_cmp_eq_u32 s35, 3\n"
    "s_cselect_b32 s71, 10, 14\n"
    "s_lshl_b32 s71, s76, s71\n"
    "s_lshl2_add_u32 s72, s75, s34\n"
    "s_lshl_b32 s72, s72, 11\n"
    "s_add_u32 s71, s71, s72\n"
    "s_add_u32 s71, s71, 65536\n"
    "v_add_u32 v54, s71, v247\n"
    "v_xor_b32 v55, 2048, v54\n"
    "v_xor_b32 v56, 4096, v54\n"
    "v_xor_b32 v57, 6144, v54\n"
    "ds_write_b128 v54, v[0:3]\n"
    "ds_write_b128 v55, v[4:7]\n"
    "ds_write_b128 v56, v[8:11]\n"
    "ds_write_b128 v57, v[12:15]\n"
    "s_cmp_eq_u32 s35, 3\n"
    "s_cbranch_scc1 .LAOW_%=\n"
    "ds_write_b128 v54, v[16:19] offset:1024\n"
    "ds_write_b128 v55, v[20:23] offset:1024\n"
    "ds_write_b128 v56, v[24:27] offset:1024\n"
    "ds_write_b128 v57, v[28:31] offset:1024\n"
    ".LAOW_%=:\n"
    "v_lshrrev_b32 v44, 4, v240\n"
    "v_xor_b32 v44, v44, v240\n"
    "v_and_b32 v44, 7, v44\n"
    "v_lshlrev_b32 v44, 4, v44\n"
    "v_lshrrev_b32 v45, 5, v240\n"
    "v_bfe_u32 v46, v240, 3, 2\n"
    "v_lshl_add_u32 v45, v45, 3, v46\n"
    "v_lshl_add_u32 v44, v45, 12, v44\n"
    "v_xor_b32 v45, 0x10040, v44\n"
    "s_lshl1_add_u32 s71, s74, 0\n"
    "s_xor_b32 s71, s71, s34\n"
    "s_lshr_b32 s72, s71, 1\n"
    "s_lshl2_add_u32 s72, s70, s72\n"
    "s_and_b32 s71, s71, 1\n"
    "s_lshl3_add_u32 s71, s72, s71\n"
    "s_and_b32 s72, s34, 1\n"
    "s_lshl3_add_u32 s71, s71, s72\n"
    "s_lshl_b32 s71, s71, 11\n"
    "v_add_u32 v32, s71, v44\n"
    "v_add_u32 v33, s71, v45\n"
    "s_lshl1_add_u32 s71, s74, 1\n"
    "s_xor_b32 s71, s71, s34\n"
    "s_lshr_b32 s72, s71, 1\n"
    "s_lshl2_add_u32 s72, s70, s72\n"
    "s_and_b32 s71, s71, 1\n"
    "s_lshl3_add_u32 s71, s72, s71\n"
    "s_and_b32 s72, s34, 1\n"
    "s_lshl3_add_u32 s71, s71, s72\n"
    "s_lshl_b32 s71, s71, 11\n"
    "v_add_u32 v34, s71, v44\n"
    "v_add_u32 v35, s71, v45\n"
    "s_lshl_b32 s71, s34, 6\n"
    "s_add_u32 s71, s71, s35\n"
    "s_xor_b32 s71, s71, 3\n"
    "s_lshl2_add_u32 s71, s71, s74\n"
    "s_lshl_b32 s71, s71, 12\n"
    "v_lshl_add_u32 v36, v240, 4, s71\n"
    "buffer_store_dwordx4 v[0:3], v247, s[60:63], s96 offen offset:0 sc1\n"
    "s_cmp_eq_u32 s35, 3\n"
    "s_cbranch_scc1 .LHS7_%=\n"
    "buffer_store_dwordx4 v[16:19], v247, s[60:63], s96 offen offset:1024 sc1\n"
    ".LHS7_%=:\n"
    "buffer_store_dwordx4 v[4:7], v247, s[60:63], s96 offen offset:2048 sc1\n"
    "s_cmp_eq_u32 s35, 3\n"
    "s_cbranch_scc1 .LHS8_%=\n"
    "buffer_store_dwordx4 v[20:23], v247, s[60:63], s96 offen offset:3072 sc1\n"
    ".LHS8_%=:\n"
    "s_mov_b32 s89, 196608\n"
    "s_barrier\n"
    "s_add_u32 m0, s91, 49152\n"
    "s_nop 0\n"
    "buffer_load_dwordx4 v36, s[60:63], s89 offen offset:0 sc1 lds\n"
    "buffer_load_dwordx4 v36, s[60:63], s89 offen offset:1024 sc1 lds\n"
    "buffer_load_dwordx4 v36, s[60:63], s89 offen offset:2048 sc1 lds\n"
    "buffer_load_dwordx4 v36, s[60:63], s89 offen offset:3072 sc1 lds\n"
    "v_readfirstlane_b32 s68, v252\n"
    "v_readfirstlane_b32 s69, v253\n"
    "s_lshl2_add_u32 s71, s30, s74\n"
    "s_lshl_b32 s71, s71, 7\n"
    "s_add_u32 s71, s71, 21102592\n"
    "v_lshl_add_u32 v48, v240, 3, s71\n"
    "s_andn2_b32 s71, s30, 0x18\n"
    "s_lshl_b32 s71, s71, 9\n"
    "s_add_u32 s71, s71, 21102592\n"
    "s_lshl_b32 s72, s75, 8\n"
    "s_add_u32 s71, s71, s72\n"
    "v_lshl_add_u32 v49, v246, 9, s71\n"
    "v_bfe_u32 v52, v240, 4, 1\n"
    "v_lshl_add_u32 v49, v52, 7, v49\n"
    "buffer_store_dwordx4 v[8:11], v247, s[60:63], s97 offen offset:0 sc1\n"
    "s_cmp_eq_u32 s35, 3\n"
    "s_cbranch_scc1 .LHS9_%=\n"
    "buffer_store_dwordx4 v[24:27], v247, s[60:63], s97 offen offset:1024 sc1\n"
    ".LHS9_%=:\n"
    "s_xor_b32 s92, s77, 4\n"
    "s_lshl_b32 s89, s92, 16\n"
    "s_barrier\n"
    "s_add_u32 m0, s91, 0\n"
    "s_nop 0\n"
    "buffer_load_dwordx4 v36, s[60:63], s89 offen offset:0 sc1 lds\n"
    "buffer_load_dwordx4 v36, s[60:63], s89 offen offset:1024 sc1 lds\n"
    "buffer_load_dwordx4 v36, s[60:63], s89 offen offset:2048 sc1 lds\n"
    "buffer_load_dwordx4 v36, s[60:63], s89 offen offset:3072 sc1 lds\n"
    "s_lshl1_add_u32 s71, s74, 0\n"
    "s_xor_b32 s71, s71, s34\n"
    "s_lshl2_add_u32 s72, s70, s34\n"
    "s_mul_i32 s72, s72, 56\n"
    "s_add_u32 s71, s71, s72\n"
    "s_lshl_b32 s71, s71, 11\n"
    "s_add_u32 s71, s71, 4194304\n"
    "v_lshl_add_u32 v37, v240, 4, s71\n"
    "s_lshl1_add_u32 s71, s74, 1\n"
    "s_xor_b32 s71, s71, s34\n"
    "s_lshl2_add_u32 s72, s70, s34\n"
    "s_mul_i32 s72, s72, 56\n"
    "s_add_u32 s71, s71, s72\n"
    "s_lshl_b32 s71, s71, 11\n"
    "s_add_u32 s71, s71, 4194304\n"
    "v_lshl_add_u32 v38, v240, 4, s71\n"
    "v_lshrrev_b32 v44, 4, v240\n"
    "v_lshlrev_b32 v45, 1, v44\n"
    "v_xor_b32 v45, v45, v240\n"
    "v_and_b32 v45, 15, v45\n"
    "v_lshlrev_b32 v45, 4, v45\n"
    "v_lshl_add_u32 v44, v44, 10, v45\n"
    "s_mul_i32 s71, s34, 56\n"
    "s_lshl2_add_u32 s71, s75, s71\n"
    "s_add_u32 s71, s71, s76\n"
    "s_lshl_b32 s71, s71, 5\n"
    "s_add_u32 s71, s71, s35\n"
    "s_xor_b32 s71, s71, 3\n"
    "s_lshl_b32 s71, s71, 8\n"
    "v_add_u32 v40, s71, v44\n"
    "v_xor_b32 v45, 16384, v44\n"
    "v_add_u32 v41, s71, v45\n"
    "v_xor_b32 v45, 4224, v44\n"
    "v_add_u32 v42, s71, v45\n"
    "v_xor_b32 v45, 20608, v44\n"
    "v_add_u32 v43, s71, v45\n"
    "buffer_store_dwordx4 v[12:15], v247, s[60:63], s97 offen offset:2048 sc1\n"
    "s_cmp_eq_u32 s35, 3\n"
    "s_cbranch_scc1 .LHS10_%=\n"
    "buffer_store_dwordx4 v[28:31], v247, s[60:63], s97 offen offset:3072 sc1\n"
    ".LHS10_%=:\n"
    "s_xor_b32 s92, s77, 4\n"
    "s_lshl_b32 s88, s92, 7\n"
    "s_barrier\n"
    "s_add_u32 m0, s91, 114688\n"
    "s_nop 0\n"
    "buffer_load_dwordx4 v32, s[52:55], s88 offen lds\n"
    "s_add_u32 m0, s91, 115712\n"
    "s_nop 0\n"
    "buffer_load_dwordx4 v33, s[52:55], s88 offen lds\n"
    "s_add_u32 m0, s91, 116736\n"
    "s_nop 0\n"
    "buffer_load_dwordx4 v34, s[52:55], s88 offen lds\n"
    "s_add_u32 m0, s91, 117760\n"
    "s_nop 0\n"
    "buffer_load_dwordx4 v35, s[52:55], s88 offen lds\n"
    "s_xor_b32 s92, s77, 5\n"
    "s_lshl_b32 s89, s92, 16\n"
    "s_waitcnt vmcnt(10)\n"
    "s_barrier\n"
    "s_add_u32 m0, s91, 16384\n"
    "s_nop 0\n"
    "buffer_load_dwordx4 v36, s[60:63], s89 offen offset:0 sc1 lds\n"
    "buffer_load_dwordx4 v36, s[60:63], s89 offen offset:1024 sc1 lds\n"
    "buffer_load_dwordx4 v36, s[60:63], s89 offen offset:2048 sc1 lds\n"
    "buffer_load_dwordx4 v36, s[60:63], s89 offen offset:3072 sc1 lds\n"
    "s_xor_b32 s92, s77, 5\n"
    "s_lshl_b32 s88, s92, 7\n"
    "s_add_u32 m0, s91, 131072\n"
    "s_nop 0\n"
    "buffer_load_dwordx4 v32, s[52:55], s88 offen lds\n"
    "s_add_u32 m0, s91, 132096\n"
    "s_nop 0\n"
    "buffer_load_dwordx4 v33, s[52:55], s88 offen lds\n"
    "s_add_u32 m0, s91, 133120\n"
    "s_nop 0\n"
    "buffer_load_dwordx4 v34, s[52:55], s88 offen lds\n"
    "s_add_u32 m0, s91, 134144\n"
    "s_nop 0\n"
    "buffer_load_dwordx4 v35, s[52:55], s88 offen lds\n"
    "s_xor_b32 s92, s77, 6\n"
    "s_lshl_b32 s89, s92, 16\n"
    "s_waitcnt vmcnt(8)\n"
    "s_barrier\n"
    "s_add_u32 m0, s91, 32768\n"
    "s_nop 0\n"
    "buffer_load_dwordx4 v36, s[60:63], s89 offen offset:0 sc1 lds\n"
    "buffer_load_dwordx4 v36, s[60:63], s89 offen offset:1024 sc1 lds\n"
    "buffer_load_dwordx4 v36, s[60:63], s89 offen offset:2048 sc1 lds\n"
    "buffer_load_dwordx4 v36, s[60:63], s89 offen offset:3072 sc1 lds\n"
    "s_xor_b32 s92, s77, 6\n"
    "s_lshl_b32 s88, s92, 7\n"
    "s_add_u32 m0, s91, 147456\n"
    "s_nop 0\n"
    "buffer_load_dwordx4 v32, s[52:55], s88 offen lds\n"
    "s_add_u32 m0, s91, 148480\n"
    "s_nop 0\n"
    "buffer_load_dwordx4 v33, s[52:55], s88 offen lds\n"
    "s_add_u32 m0, s91, 149504\n"
    "s_nop 0\n"
    "buffer_load_dwordx4 v34, s[52:55], s88 offen lds\n"
    "s_add_u32 m0, s91, 150528\n"
    "s_nop 0\n"
    "buffer_load_dwordx4 v35, s[52:55], s88 offen lds\n"
    "s_xor_b32 s92, s77, 7\n"
    "s_lshl_b32 s89, s92, 16\n"
    "s_waitcnt vmcnt(8)\n"
    "s_barrier\n"
    "s_add_u32 m0, s91, 49152\n"
    "s_nop 0\n"
    "buffer_load_dwordx4 v36, s[60:63], s89 offen offset:0 sc1 lds\n"
    "buffer_load_dwordx4 v36, s[60:63], s89 offen offset:1024 sc1 lds\n"
    "buffer_load_dwordx4 v36, s[60:63], s89 offen offset:2048 sc1 lds\n"
    "buffer_load_dwordx4 v36, s[60:63], s89 offen offset:3072 sc1 lds\n"
    "s_xor_b32 s92, s77, 7\n"
    "s_lshl_b32 s88, s92, 7\n"
    "s_add_u32 m0, s91, 98304\n"
    "s_nop 0\n"
    "buffer_load_dwordx4 v32, s[52:55], s88 offen lds\n"
    "s_add_u32 m0, s91, 99328\n"
    "s_nop 0\n"
    "buffer_load_dwordx4 v33, s[52:55], s88 offen lds\n"
    "s_add_u32 m0, s91, 100352\n"
    "s_nop 0\n"
    "buffer_load_dwordx4 v34, s[52:55], s88 offen lds\n"
    "s_add_u32 m0, s91, 101376\n"
    "s_nop 0\n"
    "buffer_load_dwordx4 v35, s[52:55], s88 offen lds\n"
    "s_xor_b32 s92, s77, 8\n"
    "s_lshl_b32 s89, s92, 16\n"
    "s_waitcnt vmcnt(8)\n"
    "s_barrier\n"
    "s_add_u32 m0, s91, 0\n"
    "s_nop 0\n"
    "buffer_load_dwordx4 v36, s[60:63], s89 offen offset:0 sc1 lds\n"
    "buffer_load_dwordx4 v36, s[60:63], s89 offen offset:1024 sc1 lds\n"
    "buffer_load_dwordx4 v36, s[60:63], s89 offen offset:2048 sc1 lds\n"
    "buffer_load_dwordx4 v36, s[60:63], s89 offen offset:3072 sc1 lds\n"
    "s_xor_b32 s92, s77, 8\n"
    "s_lshl_b32 s88, s92, 7\n"
    "s_add_u32 m0, s91, 114688\n"
    "s_nop 0\n"
    "buffer_load_dwordx4 v32, s[52:55], s88 offen lds\n"
    "s_add_u32 m0, s91, 115712\n"
    "s_nop 0\n"
    "buffer_load_dwordx4 v33, s[52:55], s88 offen lds\n"
    "s_add_u32 m0, s91, 116736\n"
    "s_nop 0\n"
    "buffer_load_dwordx4 v34, s[52:55], s88 offen lds\n"
    "s_add_u32 m0, s91, 117760\n"
    "s_nop 0\n"
    "buffer_load_dwordx4 v35, s[52:55], s88 offen lds\n"
    "s_waitcnt lgkmcnt(0)\n"
    "s_mov_b32 exec_lo, 65535\n"
    "s_mov_b32 exec_hi, 0\n"
    "buffer_store_dwordx2 v[252:253], v48, s[60:63], 0 offen sc1\n"
    "s_mov_b64 exec, -1\n"
    "s_xor_b32 s92, s77, 9\n"
    "s_lshl_b32 s89, s92, 16\n"
    "s_waitcnt vmcnt(9)\n"
    "s_barrier\n"
    "s_add_u32 m0, s91, 16384\n"
    "s_nop 0\n"
    "buffer_load_dwordx4 v36, s[60:63], s89 offen offset:0 sc1 lds\n"
    "buffer_load_dwordx4 v36, s[60:63], s89 offen offset:1024 sc1 lds\n"
    "buffer_load_dwordx4 v36, s[60:63], s89 offen offset:2048 sc1 lds\n"
    "buffer_load_dwordx4 v36, s[60:63], s89 offen offset:3072 sc1 lds\n"
    "s_xor_b32 s92, s77, 9\n"
    "s_lshl_b32 s88, s92, 7\n"
    "s_add_u32 m0, s91, 131072\n"
    "s_nop 0\n"
    "buffer_load_dwordx4 v32, s[52:55], s88 offen lds\n"
    "s_add_u32 m0, s91, 132096\n"
    "s_nop 0\n"
    "buffer_load_dwordx4 v33, s[52:55], s88 offen lds\n"
    "s_add_u32 m0, s91, 133120\n"
    "s_nop 0\n"
    "buffer_load_dwordx4 v34, s[52:55], s88 offen lds\n"
    "s_add_u32 m0, s91, 134144\n"
    "s_nop 0\n"
    "buffer_load_dwordx4 v35, s[52:55], s88 offen lds\n"
    "s_xor_b32 s92, s77, 10\n"
    "s_lshl_b32 s89, s92, 16\n"
    "s_waitcnt vmcnt(9)\n"
    "s_barrier\n"
    "s_add_u32 m0, s91, 32768\n"
    "s_nop 0\n"
    "buffer_load_dwordx4 v36, s[60:63], s89 offen offset:0 sc1 lds\n"
    "buffer_load_dwordx4 v36, s[60:63], s89 offen offset:1024 sc1 lds\n"
    "buffer_load_dwordx4 v36, s[60:63], s89 offen offset:2048 sc1 lds\n"
    "buffer_load_dwordx4 v36, s[60:63], s89 offen offset:3072 sc1 lds\n"
    "s_xor_b32 s92, s77, 10\n"
    "s_lshl_b32 s88, s92, 7\n"
    "s_add_u32 m0, s91, 147456\n"
    "s_nop 0\n"
    "buffer_load_dwordx4 v32, s[52:55], s88 offen lds\n"
    "s_add_u32 m0, s91, 148480\n"
    "s_nop 0\n"
    "buffer_load_dwordx4 v33, s[52:55], s88 offen lds\n"
    "s_add_u32 m0, s91, 149504\n"
    "s_nop 0\n"
    "buffer_load_dwordx4 v34, s[52:55], s88 offen lds\n"
    "s_add_u32 m0, s91, 150528\n"
    "s_nop 0\n"
    "buffer_load_dwordx4 v35, s[52:55], s88 offen lds\n"
    "s_xor_b32 s92, s77, 11\n"
    "s_lshl_b32 s89, s92, 16\n"
    "s_waitcnt vmcnt(8)\n"
    "s_barrier\n"
    "s_add_u32 m0, s91, 49152\n"
    "s_nop 0\n"
    "buffer_load_dwordx4 v36, s[60:63], s89 offen offset:0 sc1 lds\n"
    "buffer_load_dwordx4 v36, s[60:63], s89 offen offset:1024 sc1 lds\n"
    "buffer_load_dwordx4 v36, s[60:63], s89 offen offset:2048 sc1 lds\n"
    "buffer_load_dwordx4 v36, s[60:63], s89 offen offset:3072 sc1 lds\n"
    "s_xor_b32 s92, s77, 11\n"
    "s_lshl_b32 s88, s92, 7\n"
    "s_add_u32 m0, s91, 98304\n"
    "s_nop 0\n"
    "buffer_load_dwordx4 v32, s[52:55], s88 offen lds\n"
    "s_add_u32 m0, s91, 99328\n"
    "s_nop 0\n"
    "buffer_load_dwordx4 v33, s[52:55], s88 offen lds\n"
    "s_add_u32 m0, s91, 100352\n"
    "s_nop 0\n"
    "buffer_load_dwordx4 v34, s[52:55], s88 offen lds\n"
    "s_add_u32 m0, s91, 101376\n"
    "s_nop 0\n"
    "buffer_load_dwordx4 v35, s[52:55], s88 offen lds\n"
    "s_xor_b32 s92, s77, 12\n"
    "s_lshl_b32 s89, s92, 16\n"
    "s_waitcnt vmcnt(8)\n"
    "s_barrier\n"
    "s_add_u32 m0, s91, 0\n"
    "s_nop 0\n"
    "buffer_load_dwordx4 v36, s[60:63], s89 offen offset:0 sc1 lds\n"
    "buffer_load_dwordx4 v36, s[60:63], s89 offen offset:1024 sc1 lds\n"
    "buffer_load_dwordx4 v36, s[60:63], s89 offen offset:2048 sc1 lds\n"
    "buffer_load_dwordx4 v36, s[60:63], s89 offen offset:3072 sc1 lds\n"
    "s_xor_b32 s92, s77, 12\n"
    "s_lshl_b32 s88, s92, 7\n"
    "s_add_u32 m0, s91, 114688\n"
    "s_nop 0\n"
    "buffer_load_dwordx4 v32, s[52:55], s88 offen lds\n"
    "s_add_u32 m0, s91, 115712\n"
    "s_nop 0\n"
    "buffer_load_dwordx4 v33, s[52:55], s88 offen lds\n"
    "s_add_u32 m0, s91, 116736\n"
    "s_nop 0\n"
    "buffer_load_dwordx4 v34, s[52:55], s88 offen lds\n"
    "s_add_u32 m0, s91, 117760\n"
    "s_nop 0\n"
    "buffer_load_dwordx4 v35, s[52:55], s88 offen lds\n"
    "buffer_load_dwordx2 v[52:53], v49, s[60:63], 0 offen sc1\n"
    "s_xor_b32 s92, s77, 13\n"
    "s_lshl_b32 s89, s92, 16\n"
    "s_waitcnt vmcnt(9)\n"
    "s_barrier\n"
    "s_add_u32 m0, s91, 16384\n"
    "s_nop 0\n"
    "buffer_load_dwordx4 v36, s[60:63], s89 offen offset:0 sc1 lds\n"
    "buffer_load_dwordx4 v36, s[60:63], s89 offen offset:1024 sc1 lds\n"
    "buffer_load_dwordx4 v36, s[60:63], s89 offen offset:2048 sc1 lds\n"
    "buffer_load_dwordx4 v36, s[60:63], s89 offen offset:3072 sc1 lds\n"
    "s_xor_b32 s92, s77, 13\n"
    "s_lshl_b32 s88, s92, 7\n"
    "s_add_u32 m0, s91, 131072\n"
    "s_nop 0\n"
    "buffer_load_dwordx4 v32, s[52:55], s88 offen lds\n"
    ".LPAD6_%=:\n"
    "s_cbranch_execz .LRET_%=\n"
    "s_nop 0\n"
    "s_add_u32 m0, s91, 132096\n"
    "s_nop 0\n"
    "buffer_load_dwordx4 v33, s[52:55], s88 offen lds\n"
    "s_add_u32 m0, s91, 133120\n"
    "s_nop 0\n"
    "buffer_load_dwordx4 v34, s[52:55], s88 offen lds\n"
    "s_add_u32 m0, s91, 134144\n"
    "s_nop 0\n"
    "buffer_load_dwordx4 v35, s[52:55], s88 offen lds\n"
    "s_xor_b32 s92, s77, 14\n"
    "s_lshl_b32 s89, s92, 16\n"
    "s_waitcnt vmcnt(9)\n"
    "s_barrier\n"
    "s_add_u32 m0, s91, 32768\n"
    "s_nop 0\n"
    "buffer_load_dwordx4 v36, s[60:63], s89 offen offset:0 sc1 lds\n"
    "buffer_load_dwordx4 v36, s[60:63], s89 offen offset:1024 sc1 lds\n"
    "buffer_load_dwordx4 v36, s[60:63], s89 offen offset:2048 sc1 lds\n"
    "buffer_load_dwordx4 v36, s[60:63], s89 offen offset:3072 sc1 lds\n"
    "s_xor_b32 s92, s77, 14\n"
    "s_lshl_b32 s88, s92, 7\n"
    "s_add_u32 m0, s91, 147456\n"
    "s_nop 0\n"
    "buffer_load_dwordx4 v32, s[52:55], s88 offen lds\n"
    "s_add_u32 m0, s91, 148480\n"
    "s_nop 0\n"
    "buffer_load_dwordx4 v33, s[52:55], s88 offen lds\n"
    "s_add_u32 m0, s91, 149504\n"
    "s_nop 0\n"
    "buffer_load_dwordx4 v34, s[52:55], s88 offen lds\n"
    "s_add_u32 m0, s91, 150528\n"
    "s_nop 0\n"
    "buffer_load_dwordx4 v35, s[52:55], s88 offen lds\n"
    "s_waitcnt vmcnt(16)\n"
    "s_mov_b32 s78, 131072\n"
    "s_branch .LPO11c_%=\n"
    ".LPO11_%=:\n"
    "buffer_load_dwordx2 v[52:53], v49, s[60:63], 0 offen sc1\n"
    "s_waitcnt vmcnt(0)\n"
    ".LPO11c_%=:\n"
    "v_cmp_ne_u64_e32 vcc, s[68:69], v[52:53]\n"
    "s_cbranch_vccz .LPO11k_%=\n"
    "s_sleep 4\n"
    "s_sub_u32 s78, s78, 1\n"
    "s_cbranch_scc0 .LPO11_%=\n"
    ".LPO11k_%=:\n"
    "s_xor_b32 s92, s77, 15\n"
    "s_lshl_b32 s89, s92, 16\n"
    "s_waitcnt vmcnt(8)\n"
    "s_barrier\n"
    "s_add_u32 m0, s91, 49152\n"
    "s_nop 0\n"
    "buffer_load_dwordx4 v36, s[60:63], s89 offen offset:0 sc1 lds\n"
    "buffer_load_dwordx4 v36, s[60:63], s89 offen offset:1024 sc1 lds\n"
    "buffer_load_dwordx4 v36, s[60:63], s89 offen offset:2048 sc1 lds\n"
    "buffer_load_dwordx4 v36, s[60:63], s89 offen offset:3072 sc1 lds\n"
    "s_xor_b32 s92, s77, 15\n"
    "s_lshl_b32 s88, s92, 7\n"
    "s_add_u32 m0, s91, 98304\n"
    "s_nop 0\n"
    "buffer_load_dwordx4 v32, s[52:55], s88 offen lds\n"
    "s_add_u32 m0, s91, 99328\n"
    "s_nop 0\n"
    "buffer_load_dwordx4 v33, s[52:55], s88 offen lds\n"
    "s_add_u32 m0, s91, 100352\n"
    "s_nop 0\n"
    "buffer_load_dwordx4 v34, s[52:55], s88 offen lds\n"
    "s_add_u32 m0, s91, 101376\n"
    "s_nop 0\n"
    "buffer_load_dwordx4 v35, s[52:55], s88 offen lds\n"
    "s_lshl1_add_u32 s92, s35, 0\n"
    "s_sub_u32 s89, s92, 7\n"
    "s_min_u32 s92, s92, s89\n"
    "s_lshl_b32 s89, s92, 16\n"
    "s_waitcnt vmcnt(8)\n"
    "s_barrier\n"
    "s_add_u32 m0, s91, 0\n"
    "s_nop 0\n"
    "buffer_load_dwordx4 v40, s[56:59], s89 offen lds\n"
    "s_add_u32 m0, s91, 1024\n"
    "s_nop 0\n"
    "buffer_load_dwordx4 v41, s[56:59], s89 offen lds\n"
    "s_add_u32 m0, s91, 2048\n"
    "s_nop 0\n"
    "buffer_load_dwordx4 v42, s[56:59], s89 offen lds\n"
    "s_add_u32 m0, s91, 3072\n"
    "s_nop 0\n"
    "buffer_load_dwordx4 v43, s[56:59], s89 offen lds\n"
    "s_lshl1_add_u32 s92, s35, 1\n"
    "s_sub_u32 s89, s92, 7\n"
    "s_min_u32 s92, s92, s89\n"
    "s_lshl_b32 s89, s92, 16\n"
    "s_add_u32 m0, s91, 16384\n"
    "s_nop 0\n"
    "buffer_load_dwordx4 v40, s[56:59], s89 offen lds\n"
    "s_add_u32 m0, s91, 17408\n"
    "s_nop 0\n"
    "buffer_load_dwordx4 v41, s[56:59], s89 offen lds\n"
    "s_add_u32 m0, s91, 18432\n"
    "s_nop 0\n"
    "buffer_load_dwordx4 v42, s[56:59], s89 offen lds\n"
    "s_add_u32 m0, s91, 19456\n"
    "s_nop 0\n"
    "buffer_load_dwordx4 v43, s[56:59], s89 offen lds\n"
    "s_lshl1_add_u32 s92, s35, 1\n"
    "s_sub_u32 s88, s92, 7\n"
    "s_min_u32 s92, s92, s88\n"
    "s_lshl_b32 s88, s92, 14\n"
    "s_cmp_eq_u32 s35, 3\n"
    "s_cbranch_scc0 .LOW12_%=\n"
    "s_add_u32 m0, s91, 81920\n"
    "s_nop 0\n"
    "buffer_load_dwordx4 v37, s[60:63], s88 offen offset:0 sc1 lds\n"
    "buffer_load_dwordx4 v37, s[60:63], s88 offen offset:1024 sc1 lds\n"
    "s_add_u32 m0, s91, 83968\n"
    "s_nop 0\n"
    "buffer_load_dwordx4 v38, s[60:63], s88 offen offset:0 sc1 lds\n"
    "buffer_load_dwordx4 v38, s[60:63], s88 offen offset:1024 sc1 lds\n"
    ".LOW12_%=:\n"
    "s_lshl1_add_u32 s92, s35, 2\n"
    "s_sub_u32 s89, s92, 7\n"
    "s_min_u32 s92, s92, s89\n"
    "s_lshl_b32 s89, s92, 16\n"
    "s_waitcnt vmcnt(8)\n"
    "s_barrier\n"
    "s_add_u32 m0, s91, 32768\n"
    "s_nop 0\n"
    "buffer_load_dwordx4 v40, s[56:59], s89 offen lds\n"
    "s_add_u32 m0, s91, 33792\n"
    "s_nop 0\n"
    "buffer_load_dwordx4 v41, s[56:59], s89 offen lds\n"
    "s_add_u32 m0, s91, 34816\n"
    "s_nop 0\n"
    "buffer_load_dwordx4 v42, s[56:59], s89 offen lds\n"
    "s_add_u32 m0, s91, 35840\n"
    "s_nop 0\n"
    "buffer_load_dwordx4 v43, s[56:59], s89 offen lds\n"
    "s_lshl1_add_u32 s92, s35, 2\n"
    "s_sub_u32 s88, s92, 7\n"
    "s_min_u32 s92, s92, s88\n"
    "s_lshl_b32 s88, s92, 14\n"
    "s_add_u32 m0, s91, 114688\n"
    "s_nop 0\n"
    "buffer_load_dwordx4 v37, s[60:63], s88 offen offset:0 sc1 lds\n"
    "buffer_load_dwordx4 v37, s[60:63], s88 offen offset:1024 sc1 lds\n"
    "s_add_u32 m0, s91, 116736\n"
    "s_nop 0\n"
    "buffer_load_dwordx4 v38, s[60:63], s88 offen offset:0 sc1 lds\n"
    "buffer_load_dwordx4 v38, s[60:63], s88 offen offset:1024 sc1 lds\n"
    "s_lshl1_add_u32 s92, s35, 3\n"
    "s_sub_u32 s89, s92, 7\n"
    "s_min_u32 s92, s92, s89\n"
    "s_lshl_b32 s89, s92, 16\n"
    "s_waitcnt vmcnt(12)\n"
    "s_barrier\n"
    "s_add_u32 m0, s91, 49152\n"
    "s_nop 0\n"
    "buffer_load_dwordx4 v40, s[56:59], s89 offen lds\n"
    "s_add_u32 m0, s91, 50176\n"
    "s_nop 0\n"
    "buffer_load_dwordx4 v41, s[56:59], s89 offen lds\n"
    "s_add_u32 m0, s91, 51200\n"
    "s_nop 0\n"
    "buffer_load_dwordx4 v42, s[56:59], s89 offen lds\n"
    "s_add_u32 m0, s91, 52224\n"
    "s_nop 0\n"
    "buffer_load_dwordx4 v43, s[56:59], s89 offen lds\n"
    "s_lshl1_add_u32 s92, s35, 3\n"
    "s_sub_u32 s88, s92, 7\n"
    "s_min_u32 s92, s92, s88\n"
    "s_lshl_b32 s88, s92, 14\n"
    "s_add_u32 m0, s91, 131072\n"
    "s_nop 0\n"
    "buffer_load_dwordx4 v37, s[60:63], s88 offen offset:0 sc1 lds\n"
    "buffer_load_dwordx4 v37, s[60:63], s88 offen offset:1024 sc1 lds\n"
    "s_add_u32 m0, s91, 133120\n"
    "s_nop 0\n"
    "buffer_load_dwordx4 v38, s[60:63], s88 offen offset:0 sc1 lds\n"
    "buffer_load_dwordx4 v38, s[60:63], s88 offen offset:1024 sc1 lds\n"
    "s_lshl1_add_u32 s92, s35, 4\n"
    "s_sub_u32 s89, s92, 7\n"
    "s_min_u32 s92, s92, s89\n"
    "s_lshl_b32 s89, s92, 16\n"
    "s_waitcnt vmcnt(16)\n"
    "s_barrier\n"
    "s_add_u32 m0, s91, 0\n"
    "s_nop 0\n"
    "buffer_load_dwordx4 v40, s[56:59], s89 offen lds\n"
    "s_add_u32 m0, s91, 1024\n"
    "s_nop 0\n"
    "buffer_load_dwordx4 v41, s[56:59], s89 offen lds\n"
    "s_add_u32 m0, s91, 2048\n"
    "s_nop 0\n"
    "buffer_load_dwordx4 v42, s[56:59], s89 offen lds\n"
    "s_add_u32 m0, s91, 3072\n"
    "s_nop 0\n"
    "buffer_load_dwordx4 v43, s[56:59], s89 offen lds\n"
    "s_lshl1_add_u32 s92, s35, 4\n"
    "s_sub_u32 s88, s92, 7\n"
    "s_min_u32 s92, s92, s88\n"
    "s_lshl_b32 s88, s92, 14\n"
    "s_add_u32 m0, s91, 147456\n"
    "s_nop 0\n"
    "buffer_load_dwordx4 v37, s[60:63], s88 offen offset:0 sc1 lds\n"
    "buffer_load_dwordx4 v37, s[60:63], s88 offen offset:1024 sc1 lds\n"
    "s_add_u32 m0, s91, 149504\n"
    "s_nop 0\n"
    "buffer_load_dwordx4 v38, s[60:63], s88 offen offset:0 sc1 lds\n"
    "buffer_load_dwordx4 v38, s[60:63], s88 offen offset:1024 sc1 lds\n"
    "s_lshl1_add_u32 s92, s35, 5\n"
    "s_sub_u32 s89, s92, 7\n"
    "s_min_u32 s92, s92, s89\n"
    "s_lshl_b32 s89, s92, 16\n"
    "s_waitcnt vmcnt(16)\n"
    "s_barrier\n"
    "s_add_u32 m0, s91, 16384\n"
    "s_nop 0\n"
    "buffer_load_dwordx4 v40, s[56:59], s89 offen lds\n"
    "s_add_u32 m0, s91, 17408\n"
    "s_nop 0\n"
    "buffer_load_dwordx4 v41, s[56:59], s89 offen lds\n"
    "s_add_u32 m0, s91, 18432\n"
    "s_nop 0\n"
    "buffer_load_dwordx4 v42, s[56:59], s89 offen lds\n"
    "s_add_u32 m0, s91, 19456\n"
    "s_nop 0\n"
    "buffer_load_dwordx4 v43, s[56:59], s89 offen lds\n"
    "s_lshl1_add_u32 s92, s35, 5\n"
    "s_sub_u32 s88, s92, 7\n"
    "s_min_u32 s92, s92, s88\n"
    "s_lshl_b32 s88, s92, 14\n"
    "s_add_u32 m0, s91, 98304\n"
    "s_nop 0\n"
    "buffer_load_dwordx4 v37, s[60:63], s88 offen offset:0 sc1 lds\n"
    "buffer_load_dwordx4 v37, s[60:63], s88 offen offset:1024 sc1 lds\n"
    "s_add_u32 m0, s91, 100352\n"
    "s_nop 0\n"
    "buffer_load_dwordx4 v38, s[60:63], s88 offen offset:0 sc1 lds\n"
    "buffer_load_dwordx4 v38, s[60:63], s88 offen offset:1024 sc1 lds\n"
    "s_lshl1_add_u32 s92, s35, 6\n"
    "s_sub_u32 s89, s92, 7\n"
    "s_min_u32 s92, s92, s89\n"
    "s_lshl_b32 s89, s92, 16\n"
    "s_waitcnt vmcnt(16)\n"
    "s_barrier\n"
    "s_add_u32 m0, s91, 32768\n"
    "s_nop 0\n"
    "buffer_load_dwordx4 v40, s[56:59], s89 offen lds\n"
    "s_add_u32 m0, s91, 33792\n"
    "s_nop 0\n"
    "buffer_load_dwordx4 v41, s[56:59], s89 offen lds\n"
    "s_add_u32 m0, s91, 34816\n"
    "s_nop 0\n"
    "buffer_load_dwordx4 v42, s[56:59], s89 offen lds\n"
    "s_add_u32 m0, s91, 35840\n"
    "s_nop 0\n"
    "buffer_load_dwordx4 v43, s[56:59], s89 offen lds\n"
    "s_lshl1_add_u32 s92, s35, 6\n"
    "s_sub_u32 s88, s92, 7\n"
    "s_min_u32 s92, s92, s88\n"
    "s_lshl_b32 s88, s92, 14\n"
    "s_add_u32 m0, s91, 114688\n"
    "s_nop 0\n"
    "buffer_load_dwordx4 v37, s[60:63], s88 offen offset:0 sc1 lds\n"
    "buffer_load_dwordx4 v37, s[60:63], s88 offen offset:1024 sc1 lds\n"
    "s_add_u32 m0, s91, 116736\n"
    "s_nop 0\n"
    "buffer_load_dwordx4 v38, s[60:63], s88 offen offset:0 sc1 lds\n"
    "buffer_load_dwordx4 v38, s[60:63], s88 offen offset:1024 sc1 lds\n"
    "s_waitcnt vmcnt(16)\n"
    "s_barrier\n"
    "s_waitcnt vmcnt(8)\n"
    "s_barrier\n"
    "s_waitcnt vmcnt(0)\n"
    "s_barrier\n"
    "s_lshl1_add_u32 s71, s70, s75\n"
    "s_lshr_b32 s72, s34, 1\n"
    "s_lshl1_add_u32 s71, s71, s72\n"
    "s_and_b32 s72, s34, 1\n"
    "s_lshl3_add_u32 s71, s71, s72\n"
    "s_lshl4_add_u32 s71, s71, s35\n"
    "s_xor_b32 s71, s71, 3\n"
    "s_lshl1_add_u32 s71, s71, s76\n"
    "s_lshl_b32 s71, s71, 7\n"
    "s_add_u32 s71, s71, 16384\n"
    "v_lshrrev_b32 v84, 5, v240\n"
    "v_bfe_u32 v85, v240, 3, 2\n"
    "v_lshl_add_u32 v84, v84, 3, v85\n"
    "v_and_b32 v85, 7, v240\n"
    "v_lshl_add_u32 v84, v84, 6, v85\n"
    "v_lshl_add_u32 v80, v84, 4, s71\n"
    "v_lshrrev_b32 v84, 3, v240\n"
    "v_or_b32 v84, 8, v84\n"
    "v_and_b32 v86, 3, v240\n"
    "v_lshlrev_b32 v86, 2, v86\n"
    "v_xor_b32 v86, v86, v84\n"
    "v_lshl_add_u32 v86, v85, 5, v86\n"
    "s_lshl_b32 s71, s74, 12\n"
    "s_add_u32 s71, s71, 49152\n"
    "v_lshl_add_u32 v82, v86, 4, s71\n"
    "v_and_b32 v86, 1, v240\n"
    "v_bfe_u32 v81, v240, 1, 1\n"
    "v_lshl_add_u32 v86, v86, 1, v81\n"
    "v_lshlrev_b32 v81, 2, v86\n"
    "v_xor_b32 v84, v84, v81\n"
    "v_and_b32 v85, 4, v85\n"
    "v_or_b32 v85, v85, v86\n"
    "v_lshl_add_u32 v84, v85, 4, v84\n"
    "s_lshl_b32 s71, s74, 11\n"
    "s_add_u32 s71, s71, 65536\n"
    "v_lshl_add_u32 v81, v84, 4, s71\n"
    "s_lshl2_add_u32 s71, s30, s74\n"
    "s_lshl_b32 s71, s71, 7\n"
    "s_add_u32 s71, s71, 21233664\n"
    "v_lshl_add_u32 v49, v240, 3, s71\n"
    "s_mul_i32 s71, s30, 3\n"
    "s_add_u32 s71, s71, s74\n"
    "s_lshl_b32 s71, s71, 13\n"
    "s_add_u32 s71, s71, 12574720\n"
    "s_add_u32 s72, s71, 4096\n"
    "s_lshl_b32 s73, s74, 13\n"
    "s_mov_b32 s78, 131072\n"
    "s_cmp_eq_u32 s74, 0\n"
    "s_barrier\n"
    "s_cbranch_scc1 .LAXE_%=\n"
    "s_mov_b64 exec, 15\n"
    "v_mov_b64 v[64:65], 0\n"
    "buffer_atomic_add_x2 v[64:65], v49, s[60:63], 0 offen sc0 sc1\n"
    "s_sleep 3\n"
    ".LAXL_%=:\n"
    "v_mov_b64 v[66:67], 0\n"
    "buffer_atomic_add_x2 v[66:67], v49, s[60:63], 0 offen sc0 sc1\n"
    "s_sleep 3\n"
    "s_waitcnt vmcnt(1)\n"
    "v_cmp_ne_u64_e32 vcc, s[68:69], v[64:65]\n"
    "s_cbranch_vccz .LAXK_%=\n"
    "v_mov_b64 v[64:65], 0\n"
    "buffer_atomic_add_x2 v[64:65], v49, s[60:63], 0 offen sc0 sc1\n"
    "s_sleep 3\n"
    "s_waitcnt vmcnt(1)\n"
    "v_cmp_ne_u64_e32 vcc, s[68:69], v[66:67]\n"
    "s_cbranch_vccz .LAXK_%=\n"
    "s_sub_u32 s78, s78, 1\n"
    "s_cbranch_scc0 .LAXL_%=\n"
    ".LAXK_%=:\n"
    "s_mov_b64 exec, -1\n"
    "s_add_u32 m0, s73, 65536\n"
    "s_nop 0\n"
    "buffer_load_dwordx4 v247, s[60:63], s71 offen offset:0 sc1 lds\n"
    "buffer_load_dwordx4 v247, s[60:63], s71 offen offset:1024 sc1 lds\n"
    "buffer_load_dwordx4 v247, s[60:63], s71 offen offset:2048 sc1 lds\n"
    "buffer_load_dwordx4 v247, s[60:63], s71 offen offset:3072 sc1 lds\n"
    "s_add_u32 m0, s73, 69632\n"
    "s_nop 0\n"
    "buffer_load_dwordx4 v247, s[60:63], s72 offen offset:0 sc1 lds\n"
    "buffer_load_dwordx4 v247, s[60:63], s72 offen offset:1024 sc1 lds\n"
    "buffer_load_dwordx4 v247, s[60:63], s72 offen offset:2048 sc1 lds\n"
    "buffer_load_dwordx4 v247, s[60:63], s72 offen offset:3072 sc1 lds\n"
    "s_waitcnt vmcnt(0)\n"
    ".LAXE_%=:\n"
    "s_barrier\n"
    "ds_read_b128 v[104:107], v81 offset:8192\n"
    "ds_read_b128 v[96:99], v82\n"
    "ds_read_b128 v[100:103], v82 offset:256\n"
    "ds_read_b128 v[108:111], v81 offset:16384\n"
    "ds_read_b128 v[112:115], v81 offset:24576\n"
    "s_waitcnt lgkmcnt(4)\n"
    "v_lshlrev_b32 v116, 16, v104\n"
    "v_and_b32 v117, 0xffff0000, v104\n"
    "v_lshlrev_b32 v118, 16, v105\n"
    "v_and_b32 v119, 0xffff0000, v105\n"
    "v_lshlrev_b32 v120, 16, v106\n"
    "v_and_b32 v121, 0xffff0000, v106\n"
    "v_lshlrev_b32 v122, 16, v107\n"
    "v_and_b32 v123, 0xffff0000, v107\n"
    "s_waitcnt lgkmcnt(2)\n"
    "v_pk_add_f32 v[96:97], v[96:97], v[116:117]\n"
    "v_pk_add_f32 v[98:99], v[98:99], v[118:119]\n"
    "v_pk_add_f32 v[100:101], v[100:101], v[120:121]\n"
    "v_pk_add_f32 v[102:103], v[102:103], v[122:123]\n"
    "s_waitcnt lgkmcnt(1)\n"
    "v_lshlrev_b32 v116, 16, v108\n"
    "v_and_b32 v117, 0xffff0000, v108\n"
    "v_lshlrev_b32 v118, 16, v109\n"
    "v_and_b32 v119, 0xffff0000, v109\n"
    "v_lshlrev_b32 v120, 16, v110\n"
    "v_and_b32 v121, 0xffff0000, v110\n"
    "v_lshlrev_b32 v122, 16, v111\n"
    "v_and_b32 v123, 0xffff0000, v111\n"
    "v_pk_add_f32 v[96:97], v[96:97], v[116:117]\n"
    "v_pk_add_f32 v[98:99], v[98:99], v[118:119]\n"
    "v_pk_add_f32 v[100:101], v[100:101], v[120:121]\n"
    "v_pk_add_f32 v[102:103], v[102:103], v[122:123]\n"
    "s_waitcnt lgkmcnt(0)\n"
    "v_lshlrev_b32 v116, 16, v112\n"
    "v_and_b32 v117, 0xffff0000, v112\n"
    "v_lshlrev_b32 v118, 16, v113\n"
    "v_and_b32 v119, 0xffff0000, v113\n"
    "v_lshlrev_b32 v120, 16, v114\n"
    "v_and_b32 v121, 0xffff0000, v114\n"
    "v_lshlrev_b32 v122, 16, v115\n"
    "v_and_b32 v123, 0xffff0000, v115\n"
    "v_pk_add_f32 v[96:97], v[96:97], v[116:117]\n"
    "v_pk_add_f32 v[98:99], v[98:99], v[118:119]\n"
    "v_pk_add_f32 v[100:101], v[100:101], v[120:121]\n"
    "v_pk_add_f32 v[102:103], v[102:103], v[122:123]\n"
    "v_cvt_pk_bf16_f32 v96, v96, v97\n"
    "v_cvt_pk_bf16_f32 v97, v98, v99\n"
    "v_cvt_pk_bf16_f32 v98, v100, v101\n"
    "v_cvt_pk_bf16_f32 v99, v102, v103\n"
    "buffer_store_dwordx4 v[96:99], v80, s[64:67], 0 offen sc1\n"
    "s_or_b32 s71, s30, s74\n"
    "s_cmp_eq_u32 s71, 0\n"
    "s_cbranch_scc0 .LAEND_%=\n"
    "v_mov_b32 v48, 22020096\n"
    "s_mov_b32 exec_lo, 1\n"
    "s_mov_b32 exec_hi, 0\n"
    "buffer_store_dwordx2 v[252:253], v48, s[60:63], 0 offen sc1\n"
    "s_mov_b64 exec, -1\n"
    ".LAEND_%=:\n"
    "s_branch .LEND_%=\n"
    ".LEND_%=:\n"
    :
    : "s"(__builtin_amdgcn_kernarg_segment_ptr()), "s"(blockIdx.x), "s"(wave), "s"(lds0)
    : "v0", "v1", "v2", "v3", "v4", "v5", "v6", "v7", "v8", "v9", "v10", "v11", "v12", "v13", "v14", "v15", "v16", "v17", "v18", "v19", "v20", "v21", "v22", "v23", "v24", "v25", "v26", "v27", "v28", "v29", "v30", "v31", "v32", "v33", "v34", "v35", "v36", "v37", "v38", "v39", "v40", "v41", "v42", "v43", "v44", "v45", "v46", "v47", "v48", "v49", "v50", "v51", "v52", "v53", "v54", "v55", "v56", "v57", "v58", "v59", "v60", "v61", "v62", "v63", "v64", "v65", "v66", "v67", "v68", "v69", "v70", "v71", "v72", "v73", "v74", "v75", "v76", "v77", "v78", "v79", "v80", "v81", "v82", "v83", "v84", "v85", "v86", "v87", "v88", "v89", "v90", "v91", "v92", "v93", "v94", "v95", "v96", "v97", "v98", "v99", "v100", "v101", "v102", "v103", "v104", "v105", "v106", "v107", "v108", "v109", "v110", "v111", "v112", "v113", "v114", "v115", "v116", "v117", "v118", "v119", "v120", "v121", "v122", "v123", "v124", "v125", "v126", "v127", "v128", "v129", "v130", "v131", "v132", "v133", "v134", "v135", "v136", "v137", "v138", "v139", "v140", "v141", "v142", "v143", "v144", "v145", "v146", "v147", "v148", "v149", "v150", "v151", "v152", "v153", "v154", "v155", "v156", "v157", "v158", "v159", "v160", "v161", "v162", "v163", "v164", "v165", "v166", "v167", "v168", "v169", "v170", "v171", "v172", "v173", "v174", "v175", "v176", "v177", "v178", "v179", "v180", "v181", "v182", "v183", "v184", "v185", "v186", "v187", "v188", "v189", "v190", "v191", "v192", "v193", "v194", "v195", "v196", "v197", "v198", "v199", "v200", "v201", "v202", "v203", "v204", "v205", "v206", "v207", "v208", "v209", "v210", "v211", "v212", "v213", "v214", "v215", "v216", "v217", "v218", "v219", "v220", "v221", "v222", "v223", "v224", "v225", "v226", "v227", "v228", "v229", "v230", "v231", "v232", "v233", "v234", "v235", "v236", "v237", "v238", "v239", "v240", "v241", "v242", "v243", "v244", "v245", "v246", "v247", "v248", "v249", "v250", "v251", "v252", "v253", "v254", "v255", "s5", "s6", "s7", "s8", "s9", "s10", "s11", "s12", "s13", "s14", "s15", "s16", "s17", "s18", "s19", "s20", "s21", "s22", "s23", "s24", "s25", "s26", "s27", "s28", "s29", "s30", "s31", "s34", "s35", "s36", "s37", "s38", "s39", "s40", "s41", "s42", "s43", "s44", "s45", "s46", "s47", "s48", "s49", "s50", "s51", "s52", "s53", "s54", "s55", "s56", "s57", "s58", "s59", "s60", "s61", "s62", "s63", "s64", "s65", "s66", "s67", "s68", "s69", "s70", "s71", "s72", "s73", "s74", "s75", "s76", "s77", "s78", "s79", "s80", "s81", "s82", "s83", "s84", "s85", "s86", "s87", "s88", "s89", "s90", "s91", "s92", "s93", "s94", "s95", "s96", "s97", "s98", "s99", "vcc", "scc", "memory");
}

extern "C" void kernel_launch(void* const* d_in, const int* in_sizes, int n_in, void* d_out, int out_size, void* d_ws,
                              size_t ws_size, hipStream_t stream) {
  fused_kernel<<<256, 512, 0, stream>>>(d_in[0], d_in[1], d_in[2], d_in[3], d_in[4], d_in[6], d_in[7], d_in[8], d_in[9],
                                        d_out, d_ws);
}
```
